```python
import torch

def _softmax(x, axis=-1):
    return torch.softmax(x, dim=axis)
def _gelu(x):
    return torch.nn.functional.gelu(x, approximate="tanh")
def _silu(x):
    return torch.nn.functional.silu(x)
def _sigmoid(x):
    return torch.sigmoid(x)
def _layernorm(x, axis=-1, eps=1e-5):
    m = x.mean(dim=axis, keepdim=True)
    v = x.var(dim=axis, keepdim=True, unbiased=False)
    return (x - m) / torch.sqrt(v + eps)
def _scatter_add(tgt, idx, src, axis=0):
    return tgt.clone().index_add_(axis, idx, src)
def _segment_sum(src, seg, n):
    out = torch.zeros((n,) + src.shape[1:], dtype=src.dtype)
    return out.index_add_(0, seg, src)
def _pad(x, pw):
    flat = tuple(v for pair in reversed(pw) for v in pair)
    return torch.nn.functional.pad(x, flat)

kernel_name = 'interleave_layers__softmax_full_mha__dense_activation_mish__prompt'

def setup_inputs():

    _w = (lambda a: (lambda t: t.to(torch.bfloat16) if t.is_floating_point() else t)(torch.as_tensor(a)))
    def _A_setup():
        B, S, D = 1, 2048, 512
        return {
            "x": _w((lambda *s: torch.randn(*s, dtype=torch.bfloat16) * 0.05)(B, S, D)),
            "wqkv": _w((lambda *s: torch.randn(*s, dtype=torch.bfloat16) * 0.05)(D, 3 * D)),
            "wo": _w((lambda *s: torch.randn(*s, dtype=torch.bfloat16) * 0.05)(D, D)),
        }
    _A_in = _A_setup()

    def _B_setup():
        B, S, D, F = 1, 2048, 512, 1792
        return {
            "x": _w((lambda *s: torch.randn(*s, dtype=torch.bfloat16) * 0.05)(B, S, D)),
            "w1": _w((lambda *s: torch.randn(*s, dtype=torch.bfloat16) * 1.6)(D, F) * (D ** -0.5)),
            "b1": _w((lambda *s: torch.randn(*s, dtype=torch.bfloat16) * 0.4)(F)),
            "w2": _w((lambda *s: torch.randn(*s, dtype=torch.bfloat16) * 0.2)(F, D) * (F ** -0.5)),
        }
    _B_in = _B_setup()

    return {
        "A_x": _A_in["x"],
        "A_wqkv": _A_in["wqkv"],
        "A_wo": _A_in["wo"],
        "B_x": _B_in["x"],
        "B_w1": _B_in["w1"],
        "B_b1": _B_in["b1"],
        "B_w2": _B_in["w2"],
    }

def reference(A_x, A_wqkv, A_wo, B_x, B_w1, B_b1, B_w2):
    def _A_ref(x, wqkv, wo):
        B, S, D = x.shape
        H = 32; Dh = D // H
        qkv = (lambda x, *d: x.permute(*d))((x @ wqkv).reshape(B, S, 3, H, Dh), 2, 0, 3, 1, 4)
        q, k, v = qkv[0], qkv[1], qkv[2]
        scale = Dh ** -0.5
        scores = torch.einsum("bhqd,bhkd->bhqk", q, k) * scale
        p = _softmax(scores, axis=-1)
        o = torch.einsum("bhqk,bhkd->bhqd", p, v)
        return (lambda x, *d: x.permute(*d))(o, 0, 2, 1, 3).reshape(B, S, D) @ wo

    def _B_ref(x, w1, b1, w2):
        h = torch.einsum("bsd,df->bsf", x, w1) + b1
        sp = torch.log1p(torch.exp(-torch.abs(h))) + torch.where(h > 0, h, torch.zeros_like(h))
        act = h * torch.tanh(sp)
        return torch.einsum("bsf,fd->bsd", act, w2)

    x = A_x
    x = x + _A_ref(_layernorm(x, axis=-1), A_wqkv, A_wo)
    x = x + _B_ref(_layernorm(x, axis=-1), B_w1, B_b1, B_w2)
    x = x + _A_ref(_layernorm(x, axis=-1), A_wqkv, A_wo)
    x = x + _B_ref(_layernorm(x, axis=-1), B_w1, B_b1, B_w2)
    return x
```

```cpp
#include <hip/hip_runtime.h>
#include <hip/hip_bf16.h>
#include <stdint.h>

typedef __attribute__((ext_vector_type(8))) short bf16x8;
typedef __attribute__((ext_vector_type(4))) short s16x4;
typedef __attribute__((ext_vector_type(4))) float f32x4;
typedef __attribute__((ext_vector_type(2))) float f32x2;
typedef __attribute__((ext_vector_type(16))) float f32x16;
typedef __attribute__((ext_vector_type(4))) unsigned u32x4;
typedef __attribute__((ext_vector_type(2))) unsigned u32x2;
typedef __attribute__((ext_vector_type(2))) __bf16 bf16x2_t;
typedef __attribute__((address_space(3))) const char* lds_cptr;
typedef __attribute__((address_space(3))) char* lds_ptr;

#define DI __device__ __forceinline__

constexpr int SEQ = 2048, DM = 512, NH = 32, FF = 1792;

DI unsigned pack_bf16(float lo, float hi) {
    f32x2 v = {lo, hi};
    return __builtin_bit_cast(unsigned, __builtin_convertvector(v, bf16x2_t));
}
DI float bf_lo(unsigned u) { return __uint_as_float(u << 16); }
DI float bf_hi(unsigned u) { return __uint_as_float(u & 0xffff0000u); }
DI float dot2bf(unsigned a, unsigned b, float c) {
    return __builtin_amdgcn_fdot2_f32_bf16(__builtin_bit_cast(bf16x2_t, a), __builtin_bit_cast(bf16x2_t, b), c, false);
}
DI void glds16(unsigned voff, const void* sbase, unsigned lds_base) {
    asm volatile("s_mov_b32 m0, %2\n\ts_nop 0\n\tglobal_load_lds_dwordx4 %0, %1" ::"v"(voff), "s"(sbase), "s"(lds_base) : "memory");
}
DI s16x4 lds_tr(lds_cptr p) {
    typedef short v4i16_t __attribute__((ext_vector_type(4)));
    return __builtin_bit_cast(s16x4, __builtin_amdgcn_ds_read_tr16_b64_v4i16((__attribute__((address_space(3))) v4i16_t*)p));
}
DI bf16x8 lds_b128(lds_cptr p) { return *(const __attribute__((address_space(3))) bf16x8*)p; }
template <int N> DI void wait_vm_bar() { asm volatile("s_waitcnt vmcnt(%0) lgkmcnt(0)\n\ts_barrier" ::"i"(N) : "memory"); }

#define MFMA16(a, b, c) __builtin_amdgcn_mfma_f32_16x16x32_bf16((a), (b), (c), 0, 0, 0)
#define MFMA32(a, b, c) __builtin_amdgcn_mfma_f32_32x32x16_bf16((a), (b), (c), 0, 0, 0)

enum { EPI_QKV = 0, EPI_FFN1 = 1, EPI_RES = 2 };

template <int PITCH> DI int wswz(int row) {
    if (PITCH == 128) return ((row >> 1) & 1) | (((row >> 3) & 1) << 1);
    if (PITCH == 256) return (row & 3) | (((row >> 3) & 1) << 2);
    return (row >> 3) & 1;
}

template <int EPI, int BM, int BN, int KT, int N, int WK, int NSLOT, int TMX  >
__global__ __launch_bounds__(512) void gemm_kernel(const uint16_t* __restrict__ X, const uint16_t* __restrict__ W,
                                                   const uint16_t* __restrict__ aux,
                                                   uint16_t* __restrict__ out) {
    constexpr bool LN = (EPI != EPI_RES);
    constexpr int TN = (WK == 1) ? BN / 32 : 2;
    constexpr int PITCH = BN * 2;
    constexpr int XB = BM * 128, WB = 64 * PITCH;
    constexpr int SLOT = WK * (XB + WB);
    constexpr int NC = KT / (64 * WK);
    constexpr int DEPTH = NSLOT - 1;
    constexpr int PWTOT = WK * WB / 1024;
    constexpr int NTN = N / BN, NTM = SEQ / BM;
    constexpr int RING = NSLOT * SLOT;
    constexpr int LDS_BYTES = RING + 4096;
    static_assert(WK * XB == 16384, "x pieces per slot must be 16");
    __shared__ __attribute__((aligned(16))) char lds[LDS_BYTES];

    const int tid = threadIdx.x, lane = tid & 63;
    const int wid = __builtin_amdgcn_readfirstlane(tid >> 6);
    const int r = lane & 15, g = lane >> 4;
    int tm, tn;
    {
        const int x = blockIdx.x & 7, i = blockIdx.x >> 3;
        constexpr int XM = NTM / TMX;
        constexpr int XN = 8 / XM;
        constexpr int TNX = NTN / XN;
        tm = (x / XN) * TMX + (i % TMX);
        tn = (x % XN) * TNX + (i / TMX);
    }
    const int row0 = tm * BM, n0 = tn * BN;
    const int wk = (WK == 1) ? 0 : (wid >> 2);
    const int wm = (WK == 1) ? (wid >> 1) : ((wid >> 1) & 1);
    const int wn = wid & 1;
    const unsigned lds0 = (unsigned)(uintptr_t)(lds_ptr)lds;

    u32x2 resid[2];
    if (EPI == EPI_RES) {
#pragma unroll
        for (int j = 0; j < 2; ++j)
            resid[j] = *(const u32x2*)(aux + (size_t)(row0 + wm * 32 + wk * 16 + r) * N + n0 + wn * 32 + j * 16 + g * 4);
    }
    u32x2 bias[TN];
    if (EPI == EPI_FFN1) {
#pragma unroll
        for (int j = 0; j < TN; ++j) bias[j] = *(const u32x2*)(aux + n0 + wn * (BN / 2) + j * 16 + g * 4);
    }

    unsigned xoff[2], woff[2];
#pragma unroll
    for (int i = 0; i < 2; ++i) {
        const int b = (wid + 8 * i) * 1024 + lane * 16;
        const int sub = b / XB, bb = b % XB;
        const int row = bb >> 7, c = ((bb >> 4) & 7) ^ ((row >> 1) & 7);
        xoff[i] = (unsigned)(row * KT * 2 + sub * 128 + c * 16);
    }
#pragma unroll
    for (int i = 0; i < 2; ++i) {
        int b = (wid + 8 * i) * 1024 + lane * 16;
        if (b >= WK * WB) b = 0;
        const int sub = b / WB, bb = b % WB;
        const int row = bb / PITCH, rr = bb % PITCH;
        const int seg = (rr >> 5) ^ wswz<PITCH>(row);
        woff[i] = (unsigned)((sub * 64 + row) * N * 2 + seg * 32 + (rr & 16));
    }
    const char* xg = (const char*)(X + (size_t)row0 * KT);
    const char* wg = (const char*)(W + n0);
    const bool w2 = (wid + 8 < PWTOT);
    auto issue = [&](int c) {
        const unsigned sb = lds0 + (c % NSLOT) * SLOT + wid * 1024;
        const char* xs = xg + (size_t)c * (WK * 128);
        const char* ws = wg + (size_t)c * (WK * 64 * N * 2);
        glds16(xoff[0], xs, sb);
        glds16(xoff[1], xs, sb + 8192);
        glds16(woff[0], ws, sb + 16384);
        if (PWTOT == 16 || w2) glds16(woff[1], ws, sb + 16384 + 8192);
    };
#pragma unroll
    for (int c = 0; c < DEPTH; ++c) issue(c);

    const int xrow = wm * 32 + r;
    const lds_cptr xa0 = (lds_cptr)lds + wk * XB + xrow * 128 + (((0 + g) ^ ((xrow >> 1) & 7)) << 4);
    const lds_cptr xa1 = (lds_cptr)lds + wk * XB + xrow * 128 + (((4 + g) ^ ((xrow >> 1) & 7)) << 4);
    lds_cptr wa[TN];
    {
        const int q = (lane >> 2) & 3, p = lane & 3;
        const int wrow = 8 * g + q;
#pragma unroll
        for (int j = 0; j < TN; ++j)
            wa[j] = (lds_cptr)lds + WK * XB + wk * WB + wrow * PITCH + (((wn * TN + j) ^ wswz<PITCH>(wrow)) << 5) + p * 8;
    }

    f32x4 acc[2][TN];
#pragma unroll
    for (int m = 0; m < 2; ++m)
#pragma unroll
        for (int j = 0; j < TN; ++j) acc[m][j] = f32x4{0.f, 0.f, 0.f, 0.f};
    float xs1[2] = {0.f, 0.f}, xs2[2] = {0.f, 0.f}, wcs[TN];
#pragma unroll
    for (int j = 0; j < TN; ++j) wcs[j] = 0.f;
    const unsigned ONES = 0x3f803f80u;

#pragma unroll
    for (int c = 0; c < NC; ++c) {
        constexpr int dummy = 0; (void)dummy;
        const int rem = (NC - 1 - c) < (DEPTH - 1) ? (NC - 1 - c) : (DEPTH - 1);
        if (PWTOT == 16) {
            if (rem == 0) wait_vm_bar<0>(); else if (rem == 1) wait_vm_bar<4>(); else if (rem == 2) wait_vm_bar<8>(); else wait_vm_bar<12>();
        } else {
            if (w2) { if (rem == 0) wait_vm_bar<0>(); else if (rem == 1) wait_vm_bar<4>(); else if (rem == 2) wait_vm_bar<8>(); else wait_vm_bar<12>(); }
            else    { if (rem == 0) wait_vm_bar<0>(); else if (rem == 1) wait_vm_bar<3>(); else if (rem == 2) wait_vm_bar<6>(); else wait_vm_bar<9>(); }
        }
        if (c + DEPTH < NC) issue(c + DEPTH);
        const int so = (c % NSLOT) * SLOT;
#pragma unroll
        for (int s = 0; s < 2; ++s) {
            bf16x8 xf[2], wf[TN];
#pragma unroll
            for (int m = 0; m < 2; ++m) xf[m] = lds_b128((s ? xa1 : xa0) + so + m * 2048);
#pragma unroll
            for (int j = 0; j < TN; ++j) {
                const s16x4 lo = lds_tr(wa[j] + so + (32 * s) * PITCH);
                const s16x4 hi = lds_tr(wa[j] + so + (32 * s + 4) * PITCH);
                wf[j] = __builtin_shufflevector(lo, hi, 0, 1, 2, 3, 4, 5, 6, 7);
            }
#pragma unroll
            for (int m = 0; m < 2; ++m)
#pragma unroll
                for (int j = 0; j < TN; ++j) acc[m][j] = MFMA16(wf[j], xf[m], acc[m][j]);
            if (LN) {
                if (s == wn) {
#pragma unroll
                    for (int m = 0; m < 2; ++m) {
                        const u32x4 u = __builtin_bit_cast(u32x4, xf[m]);
#pragma unroll
                        for (int e = 0; e < 4; ++e) { xs1[m] = dot2bf(u[e], ONES, xs1[m]); xs2[m] = dot2bf(u[e], u[e], xs2[m]); }
                    }
                }
                if ((c & 3) == wm) {
#pragma unroll
                    for (int j = 0; j < TN; ++j) {
                        const u32x4 u = __builtin_bit_cast(u32x4, wf[j]);
#pragma unroll
                        for (int e = 0; e < 4; ++e) wcs[j] = dot2bf(u[e], ONES, wcs[j]);
                    }
                }
            }
        }
    }

    if (LN) {
        float* st = (float*)(lds + RING);
        float* cs = (float*)(lds + RING + 2048);
#pragma unroll
        for (int m = 0; m < 2; ++m) {
            xs1[m] += __shfl_xor(xs1[m], 16); xs1[m] += __shfl_xor(xs1[m], 32);
            xs2[m] += __shfl_xor(xs2[m], 16); xs2[m] += __shfl_xor(xs2[m], 32);
            if (g == 0) *(f32x2*)(st + (wn * 128 + wm * 32 + m * 16 + r) * 2) = f32x2{xs1[m], xs2[m]};
        }
#pragma unroll
        for (int j = 0; j < TN; ++j) {
            wcs[j] += __shfl_xor(wcs[j], 16); wcs[j] += __shfl_xor(wcs[j], 32);
            if (g == 0) cs[wm * BN + wn * (BN / 2) + j * 16 + r] = wcs[j];
        }
        __syncthreads();
        float mean[2], rstd[2];
#pragma unroll
        for (int m = 0; m < 2; ++m) {
            const f32x2 a = *(const f32x2*)(st + (wm * 32 + m * 16 + r) * 2);
            const f32x2 b = *(const f32x2*)(st + (128 + wm * 32 + m * 16 + r) * 2);
            const float mu = (a[0] + b[0]) * (1.0f / KT);
            const float var = fmaxf((a[1] + b[1]) * (1.0f / KT) - mu * mu, 0.f);
            mean[m] = mu;
            rstd[m] = __builtin_amdgcn_rsqf(var + 1e-5f);
        }
#pragma unroll
        for (int j = 0; j < TN; ++j) {
            const int nl = wn * (BN / 2) + j * 16 + g * 4;
            f32x4 c4 = *(const f32x4*)(cs + nl);
#pragma unroll
            for (int w = 1; w < 4; ++w) c4 += *(const f32x4*)(cs + w * BN + nl);
#pragma unroll
            for (int m = 0; m < 2; ++m) {
                const int tok = row0 + wm * 32 + m * 16 + r;
                float v[4];
#pragma unroll
                for (int e = 0; e < 4; ++e) v[e] = (acc[m][j][e] - mean[m] * c4[e]) * rstd[m];
                if (EPI == EPI_QKV) {
                    const int blk = (n0 + wn * (BN / 2)) / 16 + j;
                    const int sec = blk >> 5, head = blk & 31;
                    if (sec == 0) {
#pragma unroll
                        for (int e = 0; e < 4; ++e) v[e] *= 0.25f * 1.4426950408889634f;
                    }
                    const u32x2 o = {pack_bf16(v[0], v[1]), pack_bf16(v[2], v[3])};
                    char* base = (char*)out + (size_t)sec * (SEQ * DM * 2) + head * 65536;
                    if (sec < 2) *(u32x2*)(base + (tok >> 5) * 1024 + (g >> 1) * 512 + (tok & 31) * 16 + (g & 1) * 8) = o;
                    else         *(u32x2*)(base + tok * 32 + (((g & 1) << 1) | (g >> 1)) * 8) = o;
                } else {
                    const float bb[4] = {bf_lo(bias[j][0]), bf_hi(bias[j][0]), bf_lo(bias[j][1]), bf_hi(bias[j][1])};
#pragma unroll
                    for (int e = 0; e < 4; ++e) {
                        const float h = v[e] + bb[e];
                        const float ex = __builtin_amdgcn_exp2f(fminf(h, 30.f) * 1.4426950408889634f);
                        const float nn = ex * (ex + 2.f);
                        v[e] = h * nn * __builtin_amdgcn_rcpf(nn + 2.f);
                    }
                    const u32x2 o = {pack_bf16(v[0], v[1]), pack_bf16(v[2], v[3])};
                    *(u32x2*)(out + (size_t)tok * N + n0 + nl) = o;
                }
            }
        }
    } else {
        __syncthreads();
        f32x4* ex = (f32x4*)lds;
#pragma unroll
        for (int j = 0; j < 2; ++j) ex[(wid * 2 + j) * 64 + lane] = wk ? acc[0][j] : acc[1][j];
        __syncthreads();
#pragma unroll
        for (int j = 0; j < 2; ++j) {
            const f32x4 o4 = ex[((wid ^ 4) * 2 + j) * 64 + lane];
            const f32x4 mine = wk ? acc[1][j] : acc[0][j];
            const float v0 = mine[0] + o4[0] + bf_lo(resid[j][0]), v1 = mine[1] + o4[1] + bf_hi(resid[j][0]);
            const float v2 = mine[2] + o4[2] + bf_lo(resid[j][1]), v3 = mine[3] + o4[3] + bf_hi(resid[j][1]);
            const u32x2 o = {pack_bf16(v0, v1), pack_bf16(v2, v3)};
            *(u32x2*)(out + (size_t)(row0 + wm * 32 + wk * 16 + r) * N + n0 + wn * 32 + j * 16 + g * 4) = o;
        }
    }
}

constexpr int A_K = 0, A_V = 65536, A_ONE = 131072, A_Q = A_ONE + 4096, A_BYTES = A_Q + 8192;

DI float max16(const f32x16& s) {
    float a = fmaxf(fmaxf(s[0], s[1]), s[2]), b = fmaxf(fmaxf(s[3], s[4]), s[5]);
    a = fmaxf(fmaxf(a, s[6]), s[7]); b = fmaxf(fmaxf(b, s[8]), s[9]);
    a = fmaxf(fmaxf(a, s[10]), s[11]); b = fmaxf(fmaxf(b, s[12]), s[13]);
    a = fmaxf(fmaxf(a, s[14]), s[15]);
    a = fmaxf(a, b);
    auto rr = __builtin_amdgcn_permlane32_swap(__float_as_uint(a), __float_as_uint(a), false, false);
    return fmaxf(__uint_as_float(rr[0]), __uint_as_float(rr[1]));
}

__global__ __launch_bounds__(512) void attn_kernel(const uint16_t* __restrict__ QKV, uint16_t* __restrict__ O) {
    __shared__ __attribute__((aligned(16))) char lds[A_BYTES];
    const int tid = threadIdx.x, lane = tid & 63;
    const int wid = __builtin_amdgcn_readfirstlane(tid >> 6);
    const int x = blockIdx.x & 7, i = blockIdx.x >> 3;
    const int head = x * 4 + (i >> 3), qb = i & 7;
    const int r = lane & 31, h = lane >> 5;
    const unsigned lds0 = (unsigned)(uintptr_t)(lds_ptr)lds;
    const char* Qg = (const char*)QKV + head * 65536;
    const char* Kg = Qg + SEQ * DM * 2;
    const char* Vg = Kg + SEQ * DM * 2;
    const unsigned lo = lane * 16;
    glds16(lo + (qb * 8 + wid) * 1024, Qg, lds0 + A_Q + wid * 1024);
    {
        glds16(lo + wid * 1024, Kg, lds0 + A_K + wid * 1024);
        glds16(lo + wid * 1024, Vg, lds0 + A_V + wid * 1024);
        if (wid < 4) glds16(lo + (8 + wid) * 1024, Kg, lds0 + A_K + (8 + wid) * 1024);
        else         glds16(lo + (4 + wid) * 1024, Vg, lds0 + A_V + (4 + wid) * 1024);
    }
#pragma unroll
    for (int p = 0; p < 13; ++p) {
        const int id = p * 8 + wid;
        const int pc = 12 + (id >> 1);
        if (id & 1) glds16(lo + pc * 1024, Vg, lds0 + A_V + pc * 1024);
        else        glds16(lo + pc * 1024, Kg, lds0 + A_K + pc * 1024);
    }
    *(u32x2*)(lds + A_ONE + tid * 8) = u32x2{0x3f803f80u, 0x3f803f80u};

    const bool isv = (lane & 16) == 0;
    lds_cptr kp = (lds_cptr)lds + A_K + h * 512 + r * 16;
    lds_cptr vp = isv ? (lds_cptr)lds + A_V + (4 * h + ((lane >> 2) & 3)) * 32 + (lane & 3) * 8
                      : (lds_cptr)lds + A_ONE + (1 - h) * 128;
    const int vstep = isv ? 4096 : 0;

    wait_vm_bar<13>();
    const bf16x8 qf = lds_b128((lds_cptr)lds + A_Q + wid * 1024 + h * 512 + r * 16);
    f32x16 negm;
    {
        f32x16 z = {};
        const f32x16 s0 = MFMA32(lds_b128(kp), qf, z);
        const float m0 = -max16(s0);
#pragma unroll
        for (int e = 0; e < 16; ++e) negm[e] = m0;
    }
    f32x16 o = {};
    f32x16 sc = MFMA32(lds_b128(kp), qf, negm);

    auto group = [&](bool last) {
#pragma unroll
        for (int u = 0; u < 4; ++u) {
            f32x16 sn;
            if (!(last && u == 3)) sn = MFMA32(lds_b128(kp + (u + 1) * 1024), qf, negm);
            u32x4 p0, p1;
#pragma unroll
            for (int e = 0; e < 4; ++e) {
                p0[e] = pack_bf16(__builtin_amdgcn_exp2f(sc[2 * e]), __builtin_amdgcn_exp2f(sc[2 * e + 1]));
                p1[e] = pack_bf16(__builtin_amdgcn_exp2f(sc[8 + 2 * e]), __builtin_amdgcn_exp2f(sc[8 + 2 * e + 1]));
            }
            const s16x4 v00 = lds_tr(vp + u * 1024), v01 = lds_tr(vp + u * 1024 + 256);
            const s16x4 v10 = lds_tr(vp + u * 1024 + 512), v11 = lds_tr(vp + u * 1024 + 768);
            o = MFMA32(__builtin_shufflevector(v00, v01, 0, 1, 2, 3, 4, 5, 6, 7), __builtin_bit_cast(bf16x8, p0), o);
            o = MFMA32(__builtin_shufflevector(v10, v11, 0, 1, 2, 3, 4, 5, 6, 7), __builtin_bit_cast(bf16x8, p1), o);
            if (!(last && u == 3)) sc = sn;
        }
        kp += 4096;
        vp += vstep;
    };
    group(false);
    group(false);
    wait_vm_bar<0>();
#pragma unroll 1
    for (int gi = 2; gi < 15; ++gi) group(false);
    group(true);

    bool bad = false;
#pragma unroll
    for (int e = 0; e < 9; ++e) bad |= !(__builtin_fabsf(o[e]) < __builtin_inff());
    if (__any(bad)) {
        lds_cptr k2 = (lds_cptr)lds + A_K + h * 512 + r * 16;
        lds_cptr v2 = isv ? (lds_cptr)lds + A_V + (4 * h + ((lane >> 2) & 3)) * 32 + (lane & 3) * 8
                          : (lds_cptr)lds + A_ONE + (1 - h) * 128;
        const int vs2 = isv ? 1024 : 0;
        float m = -__builtin_inff();
        o = f32x16{};
#pragma unroll 1
        for (int t = 0; t < 64; ++t) {
            f32x16 z = {};
            const f32x16 s = MFMA32(lds_b128(k2), qf, z);
            const float mn = fmaxf(m, max16(s));
            const float al = __builtin_amdgcn_exp2f(m - mn);
            m = mn;
#pragma unroll
            for (int e = 0; e < 16; ++e) o[e] *= al;
            u32x4 p0, p1;
#pragma unroll
            for (int e = 0; e < 4; ++e) {
                p0[e] = pack_bf16(__builtin_amdgcn_exp2f(s[2 * e] - mn), __builtin_amdgcn_exp2f(s[2 * e + 1] - mn));
                p1[e] = pack_bf16(__builtin_amdgcn_exp2f(s[8 + 2 * e] - mn), __builtin_amdgcn_exp2f(s[8 + 2 * e + 1] - mn));
            }
            const s16x4 v00 = lds_tr(v2), v01 = lds_tr(v2 + 256), v10 = lds_tr(v2 + 512), v11 = lds_tr(v2 + 768);
            o = MFMA32(__builtin_shufflevector(v00, v01, 0, 1, 2, 3, 4, 5, 6, 7), __builtin_bit_cast(bf16x8, p0), o);
            o = MFMA32(__builtin_shufflevector(v10, v11, 0, 1, 2, 3, 4, 5, 6, 7), __builtin_bit_cast(bf16x8, p1), o);
            k2 += 1024;
            v2 += vs2;
        }
    }
    const float rl = 1.0f / o[8];
    const u32x4 ov = {pack_bf16(o[0] * rl, o[1] * rl), pack_bf16(o[2] * rl, o[3] * rl),
                      pack_bf16(o[4] * rl, o[5] * rl), pack_bf16(o[6] * rl, o[7] * rl)};
    *(u32x4*)(O + (size_t)(qb * 256 + wid * 32 + r) * DM + head * 16 + h * 8) = ov;
}

extern "C" void kernel_launch(void* const* d_in, const int* in_sizes, int n_in, void* d_out, int out_size, void* d_ws,
                              size_t ws_size, hipStream_t stream) {
    const uint16_t* x0 = (const uint16_t*)d_in[0];
    const uint16_t* wqkv = (const uint16_t*)d_in[1];
    const uint16_t* wo = (const uint16_t*)d_in[2];
    const uint16_t* w1 = (const uint16_t*)d_in[4];
    const uint16_t* b1 = (const uint16_t*)d_in[5];
    const uint16_t* w2 = (const uint16_t*)d_in[6];
    char* ws = (char*)d_ws;
    constexpr size_t MB2 = (size_t)SEQ * DM * 2;
    uint16_t* qkv = (uint16_t*)ws;
    uint16_t* ob = (uint16_t*)(ws + 3 * MB2);
    uint16_t* x1 = (uint16_t*)(ws + 4 * MB2);
    uint16_t* x2 = (uint16_t*)(ws + 5 * MB2);
    uint16_t* hb = (uint16_t*)(ws + 6 * MB2);
    const uint16_t* xin = x0;
    for (int layer = 0; layer < 2; ++layer) {
        uint16_t* xout = layer == 0 ? x2 : (uint16_t*)d_out;
        gemm_kernel<EPI_QKV, 128, 96, 512, 1536, 1, 5, 4><<<256, 512, 0, stream>>>(xin, wqkv, nullptr, qkv);
        attn_kernel<<<256, 512, 0, stream>>>(qkv, ob);
        gemm_kernel<EPI_RES, 64, 64, 512, 512, 2, 4, 8><<<256, 512, 0, stream>>>(ob, wo, xin, x1);
        gemm_kernel<EPI_FFN1, 128, 128, 512, 1792, 1, 4, 4><<<224, 512, 0, stream>>>(x1, w1, b1, hb);
        gemm_kernel<EPI_RES, 64, 64, 1792, 512, 2, 4, 8><<<256, 512, 0, stream>>>(hb, w2, x1, xout);
        xin = xout;
    }
}
```

```cpp
#include <hip/hip_runtime.h>
#include <hip/hip_bf16.h>
#include <stdint.h>

typedef __attribute__((ext_vector_type(8))) short bf16x8;
typedef __attribute__((ext_vector_type(4))) short s16x4;
typedef __attribute__((ext_vector_type(4))) float f32x4;
typedef __attribute__((ext_vector_type(2))) float f32x2;
typedef __attribute__((ext_vector_type(16))) float f32x16;
typedef __attribute__((ext_vector_type(4))) unsigned u32x4;
typedef __attribute__((ext_vector_type(2))) unsigned u32x2;
typedef __attribute__((ext_vector_type(2))) __bf16 bf16x2_t;
typedef __attribute__((address_space(3))) const char* lds_cptr;
typedef __attribute__((address_space(3))) char* lds_ptr;

#define DI __device__ __forceinline__

constexpr int SEQ = 2048, DM = 512, NH = 32, FF = 1792;

DI unsigned pack_bf16(float lo, float hi) {
    f32x2 v = {lo, hi};
    return __builtin_bit_cast(unsigned, __builtin_convertvector(v, bf16x2_t));
}
DI float bf_lo(unsigned u) { return __uint_as_float(u << 16); }
DI float bf_hi(unsigned u) { return __uint_as_float(u & 0xffff0000u); }
DI float dot2bf(unsigned a, unsigned b, float c) {
    return __builtin_amdgcn_fdot2_f32_bf16(__builtin_bit_cast(bf16x2_t, a), __builtin_bit_cast(bf16x2_t, b), c, false);
}
DI void glds16(unsigned voff, const void* sbase, unsigned lds_base) {
    asm volatile("s_mov_b32 m0, %2\n\ts_nop 0\n\tglobal_load_lds_dwordx4 %0, %1" ::"v"(voff), "s"(sbase), "s"(lds_base) : "memory");
}
DI s16x4 lds_tr(lds_cptr p) {
    typedef short v4i16_t __attribute__((ext_vector_type(4)));
    return __builtin_bit_cast(s16x4, __builtin_amdgcn_ds_read_tr16_b64_v4i16((__attribute__((address_space(3))) v4i16_t*)p));
}
DI bf16x8 lds_b128(lds_cptr p) { return *(const __attribute__((address_space(3))) bf16x8*)p; }
template <int N> DI void wait_vm_bar() {
    __builtin_amdgcn_s_waitcnt(0xC07F);
    asm volatile("s_waitcnt vmcnt(%0) lgkmcnt(0)\n\ts_barrier" ::"i"(N) : "memory");
}

DI void bar_lds() {
    __builtin_amdgcn_s_waitcnt(0xC07F);
    asm volatile("s_waitcnt lgkmcnt(0)\n\ts_barrier" ::: "memory");
}

#define MFMA16(a, b, c) __builtin_amdgcn_mfma_f32_16x16x32_bf16((a), (b), (c), 0, 0, 0)
#define MFMA32(a, b, c) __builtin_amdgcn_mfma_f32_32x32x16_bf16((a), (b), (c), 0, 0, 0)

enum { EPI_QKV = 0, EPI_FFN1 = 1, EPI_RES = 2 };
#define LD_INIT 2

template <int PB> DI int wswz(int row) {
    if (PB == 64) return (row >> 3) & 1;
    if (PB == 128) return ((row >> 1) & 1) | (((row >> 3) & 1) << 1);
    return (row & 3) | (((row >> 3) & 1) << 2);
}

template <int EPI, int BM, int BN, int KT, int N, int WK, int NSLOT, int TMX  , bool SELF>
__global__ __launch_bounds__(768) void gemm_kernel(const uint16_t* __restrict__ X, const uint16_t* __restrict__ W,
                                                   const uint16_t* __restrict__ aux,
                                                   uint16_t* __restrict__ out, float* __restrict__ stats, float oscale) {
    constexpr bool LN = (EPI != EPI_RES);
    constexpr int TN = (WK == 1) ? BN / 32 : 2;
    constexpr int PB = (BN == 96) ? 64 : BN * 2;
    constexpr int SPR = PB / 32;
    constexpr int XB = BM * 128, WB = 64 * BN * 2;
    constexpr int SLOT = WK * (XB + WB);
    constexpr int NC = KT / (64 * WK);
    constexpr int PW = WK * WB / 4096;
    constexpr int NTN = N / BN, NTM = SEQ / BM;
    constexpr int RING = NSLOT * SLOT;
    constexpr int LDS_BYTES = RING + 2048;
    static_assert(WK * XB == 16384, "x pieces per slot must be 16");
    __shared__ __attribute__((aligned(16))) char lds[LDS_BYTES];

    const int tid = threadIdx.x, lane = tid & 63;
    const int wid = __builtin_amdgcn_readfirstlane(tid >> 6);
    int tm, tn;
    {
        const int x = blockIdx.x & 7, i = blockIdx.x >> 3;
        constexpr int XM = NTM / TMX;
        constexpr int XN = 8 / XM;
        constexpr int TNX = NTN / XN;
        tm = (x / XN) * TMX + (i % TMX);
        tn = (x % XN) * TNX + (i / TMX);
    }
    const int row0 = tm * BM, n0 = tn * BN;
    const unsigned lds0 = (unsigned)(uintptr_t)(lds_ptr)lds;

    if (wid < 4) {
        constexpr int PER = 4 + PW;
        const int l = wid;
        const int xr = 8 * l + (lane >> 3);
        const unsigned xoff = (unsigned)(xr * KT * 2 + (((lane & 7) ^ ((xr >> 1) & 7)) << 4));
        constexpr int RPP = 1024 / PB;
        const int wr = RPP * l + lane / (PB / 16), wc = lane % (PB / 16);
        const unsigned woff = (unsigned)(wr * N * 2 + (((wc >> 1) ^ wswz<PB>(wr)) << 5) + ((wc & 1) << 4));
        constexpr int WSTEP = (BN == 96) ? 64 : 4 * RPP * N * 2;
        const char* xg = (const char*)(X + (size_t)row0 * KT);
        const char* wg = (const char*)(W + n0);
        auto issue = [&](int c) {
            const unsigned sb = lds0 + (c % NSLOT) * SLOT + l * 1024;
            const char* xs = xg + (size_t)c * (WK * 128);
            const char* ws = wg + (size_t)c * (WK * 64 * N * 2);
#pragma unroll
            for (int i = 0; i < 4; ++i)
                glds16(xoff, xs + ((WK == 1) ? i * 32 * KT * 2 : (i & 1) * 32 * KT * 2 + (i >> 1) * 128), sb + i * 4096);
#pragma unroll
            for (int i = 0; i < PW; ++i) glds16(woff, ws + i * WSTEP, sb + 16384 + i * 4096);
        };
#pragma unroll
        for (int c = 0; c < LD_INIT; ++c) issue(c);
#pragma unroll
        for (int b = -1; b < NC - 1; ++b) {
            constexpr auto nis = [](int bb) { int n = LD_INIT; for (int t = -1; t < bb; ++t) { int cap = t + NSLOT + 1; n = (n + 1 < cap) ? n + 1 : cap; if (n > NC) n = NC; } return n; };
            const int before = nis(b), after = nis(b + 1);
            const int rem = before - (b + 2);
            if (rem == 0) wait_vm_bar<0>(); else if (rem == 1) wait_vm_bar<PER>(); else if (rem == 2) wait_vm_bar<2 * PER>();
            else if (rem == 3) wait_vm_bar<3 * PER>(); else wait_vm_bar<4 * PER>();
#pragma unroll
            for (int c = 0; c < NC; ++c) if (c >= before && c < after) issue(c);
        }
        return;
    }

    const int cw = wid - 4;
    const int r = lane & 15, g = lane >> 4;
    const int wk = (WK == 1) ? 0 : (cw >> 2);
    const int wm = (WK == 1) ? (cw >> 1) : ((cw >> 1) & 1);
    const int wn = cw & 1;
    const int mrot = LN ? wn : wk;
    const int jrot = LN ? (wm % TN) : 0;
    int jb[TN];
#pragma unroll
    for (int j = 0; j < TN; ++j) jb[j] = (j + jrot) % TN;
    const int mytok = row0 + wm * 32 + mrot * 16 + r;

    f32x4 pst[2];
    u32x2 auxv[TN];
    if (LN && !SELF) {
        const f32x4* sp = (const f32x4*)(stats + (size_t)mytok * 32 + g * 8);
        pst[0] = sp[0]; pst[1] = sp[1];
    }
    if (EPI == EPI_FFN1) {
#pragma unroll
        for (int j = 0; j < TN; ++j) auxv[j] = *(const u32x2*)(aux + n0 + wn * (BN / 2) + jb[j] * 16 + g * 4);
    }
    if (EPI == EPI_RES) {
#pragma unroll
        for (int j = 0; j < 2; ++j) auxv[j] = *(const u32x2*)(aux + (size_t)mytok * N + n0 + wn * 32 + j * 16 + g * 4);
    }

    const int xrow = wm * 32 + r;
    const int xsw = (xrow >> 1) & 7;
    constexpr int NG = (NSLOT + 1) / 2;
    unsigned xa[NG][2][2], wa[NG][TN];
    {
        const int q = (lane >> 2) & 3, p = lane & 3;
        const int wrow = 8 * g + q;
#pragma unroll
        for (int gi = 0; gi < NG; ++gi) {
#pragma unroll
            for (int s = 0; s < 2; ++s)
#pragma unroll
                for (int m = 0; m < 2; ++m) {
                    xa[gi][s][m] = lds0 + gi * 2 * SLOT + wk * XB + (xrow + ((m ^ mrot) << 4)) * 128 + (((4 * s + g) ^ xsw) << 4);
                    asm volatile("" : "+v"(xa[gi][s][m]));
                }
#pragma unroll
            for (int j = 0; j < TN; ++j) {
                const int c16 = wn * TN + jb[j];
                wa[gi][j] = lds0 + gi * 2 * SLOT + WK * XB + wk * WB + (c16 / SPR) * (64 * PB) + wrow * PB +
                            (((c16 % SPR) ^ wswz<PB>(wrow)) << 5) + p * 8;
                asm volatile("" : "+v"(wa[gi][j]));
            }
        }
    }

    float* st = (float*)(lds + RING);
    float* cs = (float*)(lds + RING + 1024);
    auto put_stats = [&](float s1, float sq) {
        const float mu = s1 * (1.0f / KT);
        const float var = fmaxf(sq * (1.0f / KT) - mu * mu, 0.f);
        const float rs = __builtin_amdgcn_rsqf(var + 1e-5f);
        if (g == (r >> 2)) *(f32x2*)(st + (wm * 32 + mrot * 16 + r) * 2) = f32x2{mu * rs, rs};
    };

    f32x4 acc[2][TN];
#pragma unroll
    for (int m = 0; m < 2; ++m)
#pragma unroll
        for (int j = 0; j < TN; ++j) acc[m][j] = f32x4{0.f, 0.f, 0.f, 0.f};
    f32x4 xs1 = {0.f, 0.f, 0.f, 0.f}, xs2 = {0.f, 0.f, 0.f, 0.f}, wcs = {0.f, 0.f, 0.f, 0.f};
    const bf16x8 ones = {0x3f80, 0x3f80, 0x3f80, 0x3f80, 0x3f80, 0x3f80, 0x3f80, 0x3f80};

    bf16x8 xf[2][2][2], wf[2][2][TN];
    auto readf = [&](int c) {
        const int gi = (c % NSLOT) / 2, so = ((c % NSLOT) & 1) * SLOT, b = c & 1;
#pragma unroll
        for (int s = 0; s < 2; ++s) {
#pragma unroll
            for (int m = 0; m < 2; ++m) xf[b][s][m] = lds_b128((lds_cptr)(uintptr_t)xa[gi][s][m] + so);
#pragma unroll
            for (int j = 0; j < TN; ++j) {
                const s16x4 lo = lds_tr((lds_cptr)(uintptr_t)wa[gi][j] + so + (32 * s) * PB);
                const s16x4 hi = lds_tr((lds_cptr)(uintptr_t)wa[gi][j] + so + (32 * s + 4) * PB);
                wf[b][s][j] = __builtin_shufflevector(lo, hi, 0, 1, 2, 3, 4, 5, 6, 7);
            }
        }
    };
    auto fold_mfma = [&](int b) {
#pragma unroll
        for (int s = 0; s < 2; ++s) {
            if (LN && SELF) {
                xs1 = MFMA16(ones, xf[b][s][0], xs1);
                xs2 = MFMA16(xf[b][s][0], xf[b][s][0], xs2);
            }
            if (LN) wcs = MFMA16(wf[b][s][0], ones, wcs);
        }
    };
    auto main_mfma = [&](int b) {
#pragma unroll
        for (int s = 0; s < 2; ++s)
#pragma unroll
            for (int m = 0; m < 2; ++m)
#pragma unroll
                for (int j = 0; j < TN; ++j) acc[m][j] = MFMA16(wf[b][s][j], xf[b][s][m], acc[m][j]);
    };
    bar_lds();
    readf(0);
#pragma unroll
    for (int c = 0; c < NC - 1; ++c) {
        bar_lds();
        __builtin_amdgcn_sched_barrier(0);
        readf(c + 1);
        main_mfma(c & 1);
        fold_mfma(c & 1);
        constexpr int NM = 2 * (2 * TN + (LN ? (SELF ? 3 : 1) : 0)), NR = 2 * (2 + 2 * TN);
        constexpr int RPM = (WK == 1) ? 2 : 3;
#pragma unroll
        for (int i = 0; i < NM; ++i) {
            __builtin_amdgcn_sched_group_barrier(0x008, 1, 0);
            if (i * RPM < NR) __builtin_amdgcn_sched_group_barrier(0x100, RPM, 0);
        }
        __builtin_amdgcn_sched_barrier(0);
        if (LN && !SELF && c == NC - 3) {
            float s1 = (pst[0][0] + pst[0][2]) + (pst[1][0] + pst[1][2]);
            float sq = (pst[0][1] + pst[0][3]) + (pst[1][1] + pst[1][3]);
            s1 += __shfl_xor(s1, 16); sq += __shfl_xor(sq, 16);
            s1 += __shfl_xor(s1, 32); sq += __shfl_xor(sq, 32);
            put_stats(s1, sq);
            __builtin_amdgcn_sched_barrier(0);
        }
    }
    fold_mfma((NC - 1) & 1);
    __builtin_amdgcn_sched_barrier(0);
    main_mfma((NC - 1) & 1);
    if (LN) {
        if (SELF) put_stats(xs1[0], (r & 2) ? ((r & 1) ? xs2[3] : xs2[2]) : ((r & 1) ? xs2[1] : xs2[0]));
        if (r == 0 && wm < TN) *(f32x4*)(cs + wn * (BN / 2) + jrot * 16 + g * 4) = wcs;
    }

    if (LN) {
        bar_lds();
        f32x2 ms[2];
#pragma unroll
        for (int m = 0; m < 2; ++m) ms[m] = *(const f32x2*)(st + (wm * 32 + ((m ^ mrot) << 4) + r) * 2);
#pragma unroll
        for (int j = 0; j < TN; ++j) {
            const int nl = wn * (BN / 2) + jb[j] * 16 + g * 4;
            const f32x4 c4 = *(const f32x4*)(cs + nl);
            if (EPI == EPI_QKV) {
                const int blk = (n0 + wn * (BN / 2)) / 16 + jb[j];
                const int sec = blk >> 5, head = blk & 31;
                const float sc = sec == 0 ? 0.25f * 1.4426950408889634f : 1.0f;
                char* base = (char*)out + (size_t)sec * (SEQ * DM * 2) + head * 65536;
#pragma unroll
                for (int m = 0; m < 2; ++m) {
                    const int tok = row0 + wm * 32 + ((m ^ mrot) << 4) + r;
                    const float a = ms[m][1] * sc, bq = ms[m][0] * sc;
                    float v[4];
#pragma unroll
                    for (int e = 0; e < 4; ++e) v[e] = acc[m][j][e] * a - bq * c4[e];
                    const u32x2 o = {pack_bf16(v[0], v[1]), pack_bf16(v[2], v[3])};
                    const int off = sec < 2 ? (tok >> 5) * 1024 + (g >> 1) * 512 + (tok & 31) * 16 + (g & 1) * 8
                                            : tok * 32 + (((g & 1) << 1) | (g >> 1)) * 8;
                    *(u32x2*)(base + off) = o;
                }
            } else {
                constexpr float L2E = 1.4426950408889634f;
                const float bb[4] = {bf_lo(auxv[j][0]) * L2E, bf_hi(auxv[j][0]) * L2E, bf_lo(auxv[j][1]) * L2E, bf_hi(auxv[j][1]) * L2E};
#pragma unroll
                for (int m = 0; m < 2; ++m) {
                    const int tok = row0 + wm * 32 + ((m ^ mrot) << 4) + r;
                    const float a = ms[m][1] * L2E, bq = ms[m][0] * L2E;
                    float v[4];
#pragma unroll
                    for (int e = 0; e < 4; ++e) {
                        const float u = fmaf(acc[m][j][e], a, fmaf(-bq, c4[e], bb[e]));
                        const float ex = __builtin_amdgcn_exp2f(u);
                        const float d = fmaf(ex, ex + 2.f, 2.f);
                        v[e] = fmaf(-2.f, u * __builtin_amdgcn_rcpf(d), u);
                    }
                    const u32x2 o = {pack_bf16(v[0], v[1]), pack_bf16(v[2], v[3])};
                    *(u32x2*)(out + (size_t)tok * N + n0 + nl) = o;
                }
            }
        }
    } else {
        f32x4* ex = (f32x4*)(lds + ((NC - 2) % NSLOT) * SLOT);
#pragma unroll
        for (int j = 0; j < 2; ++j) ex[(cw * 2 + j) * 64 + lane] = acc[1][j];
        bar_lds();
        float s1 = 0.f, s2 = 0.f;
#pragma unroll
        for (int j = 0; j < 2; ++j) {
            const f32x4 o4 = ex[((cw ^ 4) * 2 + j) * 64 + lane];
            const f32x4 mine = acc[0][j];
            const float v0 = fmaf(mine[0] + o4[0], oscale, bf_lo(auxv[j][0])), v1 = fmaf(mine[1] + o4[1], oscale, bf_hi(auxv[j][0]));
            const float v2 = fmaf(mine[2] + o4[2], oscale, bf_lo(auxv[j][1])), v3 = fmaf(mine[3] + o4[3], oscale, bf_hi(auxv[j][1]));
            const u32x2 o = {pack_bf16(v0, v1), pack_bf16(v2, v3)};
            *(u32x2*)(out + (size_t)mytok * N + n0 + wn * 32 + j * 16 + g * 4) = o;
            const float q0 = bf_lo(o[0]), q1 = bf_hi(o[0]), q2 = bf_lo(o[1]), q3 = bf_hi(o[1]);
            s1 += (q0 + q1) + (q2 + q3);
            s2 = fmaf(q0, q0, fmaf(q1, q1, fmaf(q2, q2, fmaf(q3, q3, s2))));
        }
        if (!SELF) {
            s1 += __shfl_xor(s1, 16); s2 += __shfl_xor(s2, 16);
            s1 += __shfl_xor(s1, 32); s2 += __shfl_xor(s2, 32);
            if (g == 0) *(f32x2*)(stats + (size_t)mytok * 32 + (tn * 2 + wn) * 2) = f32x2{s1, s2};
        }
    }
}

constexpr int A_K = 0, A_V = 65536, A_ONE = 131072, A_Q = A_ONE + 8192, A_BYTES = A_Q + 8192;

DI float max16(const f32x16& s) {
    float a = fmaxf(fmaxf(s[0], s[1]), s[2]), b = fmaxf(fmaxf(s[3], s[4]), s[5]);
    a = fmaxf(fmaxf(a, s[6]), s[7]); b = fmaxf(fmaxf(b, s[8]), s[9]);
    a = fmaxf(fmaxf(a, s[10]), s[11]); b = fmaxf(fmaxf(b, s[12]), s[13]);
    a = fmaxf(fmaxf(a, s[14]), s[15]);
    a = fmaxf(a, b);
    auto rr = __builtin_amdgcn_permlane32_swap(__float_as_uint(a), __float_as_uint(a), false, false);
    return fmaxf(__uint_as_float(rr[0]), __uint_as_float(rr[1]));
}

#define A_EXP(S, i) "v_exp_f32 v[" #S "+" #i "], v[" #S "+" #i "]\n\t"
#define A_CVT(P, S, i) "v_cvt_pk_bf16_f32 v[" #P "], v[" #S "+" #i "], v[" #S "+" #i "+1]\n\t"
#define A_TILE(SC, SN, U)                                                                              \
    "s_waitcnt lgkmcnt(4)\n\t"                                                                         \
    "v_mfma_f32_32x32x16_bf16 v[" #SN ":" #SN "+15], v[136:139], %[q], %[nm]\n\t"                      \
    "ds_read_b128 v[136:139], %[ka] offset:(" #U "+2)*1024\n\t"                                        \
    A_EXP(SC, 0) A_EXP(SC, 1) A_EXP(SC, 2) A_EXP(SC, 3) A_EXP(SC, 4) A_EXP(SC, 5) A_EXP(SC, 6) A_EXP(SC, 7) \
    A_CVT(128, SC, 0) A_CVT(129, SC, 2) A_CVT(130, SC, 4) A_CVT(131, SC, 6)                            \
    A_EXP(SC, 8) A_EXP(SC, 9)                                                                          \
    "s_waitcnt lgkmcnt(3)\n\t"                                                                         \
    "v_mfma_f32_32x32x16_bf16 %[o], v[140:143], v[128:131], %[o]\n\t"                                  \
    "ds_read_b64_tr_b16 v[140:141], %[va] offset:(" #U "+1)*1024\n\t"                                  \
    "ds_read_b64_tr_b16 v[142:143], %[va] offset:(" #U "+1)*1024+256\n\t"                              \
    A_EXP(SC, 10) A_EXP(SC, 11) A_EXP(SC, 12) A_EXP(SC, 13) A_EXP(SC, 14) A_EXP(SC, 15)                \
    A_CVT(132, SC, 8) A_CVT(133, SC, 10) A_CVT(134, SC, 12) A_CVT(135, SC, 14)                         \
    "s_waitcnt lgkmcnt(3)\n\t"                                                                         \
    "s_nop 0\n\t"                                                                                      \
    "v_mfma_f32_32x32x16_bf16 %[o], v[144:147], v[132:135], %[o]\n\t"                                  \
    "ds_read_b64_tr_b16 v[144:145], %[va] offset:(" #U "+1)*1024+512\n\t"                              \
    "ds_read_b64_tr_b16 v[146:147], %[va] offset:(" #U "+1)*1024+768\n\t"

template <int ROWS, int CH> DI void warm_half(const uint16_t* w, int half, int t, unsigned lds_dummy) {
    static_assert((ROWS * CH) % 4096 == 0, "whole rounds only");
    const char* base = (const char*)w + half * (CH * 16);
#pragma unroll 1
    for (int k = 0; k < ROWS * CH / 4096; ++k) {
        const int idx = t + k * 4096;
        glds16((unsigned)(idx / CH) * (CH * 32) + (unsigned)(idx % CH) * 16, base, lds_dummy);
    }
}

__global__ __launch_bounds__(640) void attn_kernel(const uint16_t* __restrict__ QKV, uint16_t* __restrict__ O,
                                                   const uint16_t* __restrict__ wo, const uint16_t* __restrict__ w1,
                                                   const uint16_t* __restrict__ w2) {
    __shared__ __attribute__((aligned(16))) char lds[A_BYTES];
    const int tid = threadIdx.x, lane = tid & 63;
    const int wid = __builtin_amdgcn_readfirstlane(tid >> 6);
    const int x = blockIdx.x & 7, i = blockIdx.x >> 3;
    const int head = x * 4 + (i >> 3), qb = i & 7;
    const int r = lane & 31, h = lane >> 5;
    const unsigned lds0 = (unsigned)(uintptr_t)(lds_ptr)lds;
    const char* Qg = (const char*)QKV + head * 65536;
    const unsigned lo = lane * 16;
    if (wid >= 8) {
        const char* kv = Qg + (size_t)(wid - 7) * (SEQ * DM * 2);
        const unsigned dst = lds0 + (wid - 8) * 65536;
        asm volatile("s_barrier" ::: "memory");
#pragma unroll 4
        for (int p = 12; p < 64; ++p) glds16(lo, kv + p * 1024, dst + p * 1024);
        asm volatile("s_waitcnt vmcnt(0)\n\ts_barrier" ::: "memory");
        const int t = (i * 2 + (wid - 8)) * 64 + lane;
        const unsigned dummy = lds0 + A_Q + (wid - 8) * 1024;
        warm_half<512, 32>(wo, x & 1, t, dummy);
        warm_half<512, 112>(w1, x & 1, t, dummy);
        warm_half<1792, 32>(w2, x & 1, t, dummy);
        asm volatile("s_waitcnt vmcnt(0)" ::: "memory");
        return;
    }
    {
        const char* kv = Qg + (size_t)(1 + (wid & 1)) * (SEQ * DM * 2) + (wid >> 1) * 1024;
        const unsigned dst = lds0 + (wid & 1) * 65536 + (wid >> 1) * 1024;
        glds16(lo, Qg + (qb * 8 + wid) * 1024, lds0 + A_Q + wid * 1024);
#pragma unroll
        for (int p = 0; p < 3; ++p) glds16(lo, kv + p * 4096, dst + p * 4096);
    }
    *(u32x4*)(lds + A_ONE + tid * 16) = u32x4{(tid & 7) == 0 ? 0x00003f80u : 0u, 0u, 0u, 0u};

    const bool isv = (lane & 16) == 0;
    const unsigned ka0 = lds0 + A_K + h * 512 + r * 16;
    const unsigned va0 = isv ? lds0 + A_V + (4 * h + ((lane >> 2) & 3)) * 32 + (lane & 3) * 8
                             : lds0 + A_ONE + (1 - h) * 128 + ((lane & 3) ? 8 : 0);

    wait_vm_bar<0>();
    const bf16x8 qf = lds_b128((lds_cptr)lds + A_Q + wid * 1024 + h * 512 + r * 16);
    f32x16 negm;
    {
        f32x16 z = {};
        const f32x16 s0 = MFMA32(lds_b128((lds_cptr)(uintptr_t)ka0), qf, z);
        const float m0 = -max16(s0);
#pragma unroll
        for (int e = 0; e < 16; ++e) negm[e] = m0;
    }
    f32x16 o = {};
    {
        unsigned ka = ka0, va = va0;
        const unsigned vstep = isv ? 4096u : 0u;
        int it;
        asm volatile(
            "ds_read_b128 v[136:139], %[ka]\n\t"
            "s_mov_b32 %[it], 0\n\t"
            "s_waitcnt lgkmcnt(0)\n\t"
            "v_mfma_f32_32x32x16_bf16 v[96:111], v[136:139], %[q], %[nm]\n\t"
            "ds_read_b128 v[136:139], %[ka] offset:1024\n\t"
            "ds_read_b64_tr_b16 v[140:141], %[va]\n\t"
            "ds_read_b64_tr_b16 v[142:143], %[va] offset:256\n\t"
            "ds_read_b64_tr_b16 v[144:145], %[va] offset:512\n\t"
            "ds_read_b64_tr_b16 v[146:147], %[va] offset:768\n\t"
            "s_nop 7\n\t"
            "1:\n\t"
            A_TILE(96, 112, 0) A_TILE(112, 96, 1) A_TILE(96, 112, 2) A_TILE(112, 96, 3)
            "v_add_u32 %[ka], 0x1000, %[ka]\n\t"
            "v_add_u32 %[va], %[vs], %[va]\n\t"
            "s_add_u32 %[it], %[it], 1\n\t"
            "s_cmp_lg_u32 %[it], 2\n\t"
            "s_cbranch_scc1 2f\n\t"
            "s_barrier\n\t"
            "2:\n\t"
            "s_cmp_lg_u32 %[it], 16\n\t"
            "s_cbranch_scc1 1b\n\t"
            "s_waitcnt lgkmcnt(0)\n\t"
            "s_nop 15\n\t"
            : [o] "+v"(o), [ka] "+v"(ka), [va] "+v"(va), [it] "=&s"(it)
            : [q] "v"(qf), [nm] "v"(negm), [vs] "v"(vstep)
            : "memory", "scc", "v96", "v97", "v98", "v99", "v100", "v101", "v102", "v103", "v104", "v105", "v106", "v107",
              "v108", "v109", "v110", "v111", "v112", "v113", "v114", "v115", "v116", "v117", "v118", "v119", "v120", "v121",
              "v122", "v123", "v124", "v125", "v126", "v127", "v128", "v129", "v130", "v131", "v132", "v133", "v134", "v135",
              "v136", "v137", "v138", "v139", "v140", "v141", "v142", "v143", "v144", "v145", "v146", "v147");
    }

    bool bad = false;
#pragma unroll
    for (int e = 0; e < 9; ++e) bad |= !(__builtin_fabsf(o[e]) < __builtin_inff());
    if (__any(bad)) {
        lds_cptr k2 = (lds_cptr)(uintptr_t)ka0;
        lds_cptr v2 = (lds_cptr)(uintptr_t)va0;
        const int vs2 = isv ? 1024 : 0;
        float m = -__builtin_inff();
        o = f32x16{};
#pragma unroll 1
        for (int t = 0; t < 64; ++t) {
            f32x16 z = {};
            const f32x16 s = MFMA32(lds_b128(k2), qf, z);
            const float mn = fmaxf(m, max16(s));
            const float al = __builtin_amdgcn_exp2f(m - mn);
            m = mn;
#pragma unroll
            for (int e = 0; e < 16; ++e) o[e] *= al;
            u32x4 p0, p1;
#pragma unroll
            for (int e = 0; e < 4; ++e) {
                p0[e] = pack_bf16(__builtin_amdgcn_exp2f(s[2 * e] - mn), __builtin_amdgcn_exp2f(s[2 * e + 1] - mn));
                p1[e] = pack_bf16(__builtin_amdgcn_exp2f(s[8 + 2 * e] - mn), __builtin_amdgcn_exp2f(s[8 + 2 * e + 1] - mn));
            }
            const s16x4 v00 = lds_tr(v2), v01 = lds_tr(v2 + 256), v10 = lds_tr(v2 + 512), v11 = lds_tr(v2 + 768);
            o = MFMA32(__builtin_shufflevector(v00, v01, 0, 1, 2, 3, 4, 5, 6, 7), __builtin_bit_cast(bf16x8, p0), o);
            o = MFMA32(__builtin_shufflevector(v10, v11, 0, 1, 2, 3, 4, 5, 6, 7), __builtin_bit_cast(bf16x8, p1), o);
            k2 += 1024;
            v2 += vs2;
        }
    }
    auto lsw = __builtin_amdgcn_permlane32_swap(__float_as_uint(o[8]), __float_as_uint(o[8]), false, false);
    const float rl = 1.0f / __uint_as_float(lsw[0]);
    const u32x4 ov = {pack_bf16(o[0] * rl, o[1] * rl), pack_bf16(o[2] * rl, o[3] * rl),
                      pack_bf16(o[4] * rl, o[5] * rl), pack_bf16(o[6] * rl, o[7] * rl)};
    *(u32x4*)(O + (size_t)(qb * 256 + wid * 32 + r) * DM + head * 16 + h * 8) = ov;
}

extern "C" void kernel_launch(void* const* d_in, const int* in_sizes, int n_in, void* d_out, int out_size, void* d_ws,
                              size_t ws_size, hipStream_t stream) {
    const uint16_t* x0 = (const uint16_t*)d_in[0];
    const uint16_t* wqkv = (const uint16_t*)d_in[1];
    const uint16_t* wo = (const uint16_t*)d_in[2];
    const uint16_t* w1 = (const uint16_t*)d_in[4];
    const uint16_t* b1 = (const uint16_t*)d_in[5];
    const uint16_t* w2 = (const uint16_t*)d_in[6];
    char* ws = (char*)d_ws;
    constexpr size_t MB2 = (size_t)SEQ * DM * 2;
    uint16_t* qkv = (uint16_t*)ws;
    uint16_t* ob = (uint16_t*)(ws + 3 * MB2);
    uint16_t* x1 = (uint16_t*)(ws + 4 * MB2);
    uint16_t* x2 = (uint16_t*)(ws + 5 * MB2);
    uint16_t* hb = (uint16_t*)(ws + 6 * MB2);
    float* st1 = (float*)(ws + 10 * MB2);
    float* st2 = (float*)(ws + 11 * MB2);
    constexpr float LN2 = 0.6931471805599453f;
    gemm_kernel<EPI_QKV, 128, 96, 512, 1536, 1, 5, 4, true><<<256, 768, 0, stream>>>(x0, wqkv, nullptr, qkv, nullptr, 1.0f);
    attn_kernel<<<256, 640, 0, stream>>>(qkv, ob, wo, w1, w2);
    gemm_kernel<EPI_RES, 64, 64, 512, 512, 2, 4, 8, false><<<256, 768, 0, stream>>>(ob, wo, x0, x1, st1, 1.0f);
    gemm_kernel<EPI_FFN1, 128, 128, 512, 1792, 1, 4, 4, false><<<224, 768, 0, stream>>>(x1, w1, b1, hb, st1, 1.0f);
    gemm_kernel<EPI_RES, 64, 64, 1792, 512, 2, 4, 8, false><<<256, 768, 0, stream>>>(hb, w2, x1, x2, st2, LN2);
    gemm_kernel<EPI_QKV, 128, 96, 512, 1536, 1, 5, 4, false><<<256, 768, 0, stream>>>(x2, wqkv, nullptr, qkv, st2, 1.0f);
    attn_kernel<<<256, 640, 0, stream>>>(qkv, ob, wo, w1, w2);
    gemm_kernel<EPI_RES, 64, 64, 512, 512, 2, 4, 8, false><<<256, 768, 0, stream>>>(ob, wo, x2, x1, st1, 1.0f);
    gemm_kernel<EPI_FFN1, 128, 128, 512, 1792, 1, 4, 4, false><<<224, 768, 0, stream>>>(x1, w1, b1, hb, st1, 1.0f);
    gemm_kernel<EPI_RES, 64, 64, 1792, 512, 2, 4, 8, true><<<256, 768, 0, stream>>>(hb, w2, x1, (uint16_t*)d_out, st2, LN2);
}
```

```cpp
#include <hip/hip_runtime.h>
#include <hip/hip_bf16.h>
#include <stdint.h>

typedef __attribute__((ext_vector_type(8))) short bf16x8;
typedef __attribute__((ext_vector_type(4))) short s16x4;
typedef __attribute__((ext_vector_type(4))) float f32x4;
typedef __attribute__((ext_vector_type(2))) float f32x2;
typedef __attribute__((ext_vector_type(16))) float f32x16;
typedef __attribute__((ext_vector_type(4))) unsigned u32x4;
typedef __attribute__((ext_vector_type(2))) unsigned u32x2;
typedef __attribute__((ext_vector_type(2))) __bf16 bf16x2_t;
typedef __attribute__((address_space(3))) const char* lds_cptr;
typedef __attribute__((address_space(3))) char* lds_ptr;

#define DI __device__ __forceinline__
#define GAS __attribute__((address_space(1)))

constexpr int SEQ = 2048, DM = 512, NH = 32, FF = 1792;
constexpr int LDA = 576;

DI unsigned pack_bf16(float lo, float hi) {
    f32x2 v = {lo, hi};
    return __builtin_bit_cast(unsigned, __builtin_convertvector(v, bf16x2_t));
}
DI float bf_lo(unsigned u) { return __uint_as_float(u << 16); }
DI float bf_hi(unsigned u) { return __uint_as_float(u & 0xffff0000u); }
DI float dot2bf(unsigned a, unsigned b, float c) {
    return __builtin_amdgcn_fdot2_f32_bf16(__builtin_bit_cast(bf16x2_t, a), __builtin_bit_cast(bf16x2_t, b), c, false);
}
DI void glds16(unsigned voff, const void* sbase, unsigned lds_base) {
    asm volatile("s_mov_b32 m0, %2\n\ts_nop 0\n\tglobal_load_lds_dwordx4 %0, %1" ::"v"(voff), "s"(sbase), "s"(lds_base) : "memory");
}
DI void store16_wt(void* sbase, unsigned voff, u32x4 v) {
    asm volatile("global_store_dwordx4 %0, %1, %2 sc1\n\ts_nop 1" ::"v"(voff), "v"(v), "s"(sbase) : "memory");
}
DI u32x4 widen16(u32x2 a, u32x2 b) {
    auto x = __builtin_amdgcn_permlane16_swap(a[0], b[0], false, false);
    auto y = __builtin_amdgcn_permlane16_swap(a[1], b[1], false, false);
    return u32x4{x[0], y[0], x[1], y[1]};
}
DI float sum_rows(float v) {
    auto a = __builtin_amdgcn_permlane16_swap(__float_as_uint(v), __float_as_uint(v), false, false);
    v = __uint_as_float(a[0]) + __uint_as_float(a[1]);
    auto b = __builtin_amdgcn_permlane32_swap(__float_as_uint(v), __float_as_uint(v), false, false);
    return __uint_as_float(b[0]) + __uint_as_float(b[1]);
}
DI s16x4 lds_tr(lds_cptr p) {
    typedef short v4i16_t __attribute__((ext_vector_type(4)));
    return __builtin_bit_cast(s16x4, __builtin_amdgcn_ds_read_tr16_b64_v4i16((__attribute__((address_space(3))) v4i16_t*)p));
}
DI bf16x8 lds_b128(lds_cptr p) { return *(const __attribute__((address_space(3))) bf16x8*)p; }
template <int N> DI void wait_vm_bar() {
    __builtin_amdgcn_s_waitcnt(0xC07F);
    asm volatile("s_waitcnt vmcnt(%0) lgkmcnt(0)\n\ts_barrier" ::"i"(N) : "memory");
}

DI void bar_lds() {
    __builtin_amdgcn_s_waitcnt(0xC07F);
    asm volatile("s_waitcnt lgkmcnt(0)\n\ts_barrier" ::: "memory");
}

#define MFMA16(a, b, c) __builtin_amdgcn_mfma_f32_16x16x32_bf16((a), (b), (c), 0, 0, 0)
#define MFMA32(a, b, c) __builtin_amdgcn_mfma_f32_32x32x16_bf16((a), (b), (c), 0, 0, 0)

enum { EPI_QKV = 0, EPI_FFN1 = 1, EPI_RES = 2 };
#define LD_INIT 2

template <int PB> DI int wswz(int row) {
    if (PB == 64) return (row >> 3) & 1;
    if (PB == 128) return ((row >> 1) & 1) | (((row >> 3) & 1) << 1);
    return (row & 3) | (((row >> 3) & 1) << 2);
}

template <int EPI, int BM, int BN, int KT, int N, int WK, int NSLOT, int TMX  , bool SELF, bool CSIN>
__global__ __launch_bounds__(768) void gemm_kernel(const uint16_t* X, const uint16_t* W,
                                                   const uint16_t* aux,
                                                   uint16_t* out, float* stats, float* csg, float oscale,
                                                   int xp, int rp, int op) {
    asm volatile("" : "+s"(X), "+s"(W), "+s"(aux), "+s"(out), "+s"(stats), "+s"(csg), "+s"(oscale), "+s"(xp), "+s"(rp), "+s"(op));
    constexpr bool LN = (EPI != EPI_RES);
    constexpr int WP = LN ? N : BN;
    constexpr int TN = (WK == 1) ? BN / 32 : 2;
    constexpr int PB = (BN == 96) ? 64 : BN * 2;
    constexpr int SPR = PB / 32;
    constexpr int XB = BM * 128, WB = 64 * BN * 2;
    constexpr int SLOT = WK * (XB + WB);
    constexpr int NC = KT / (64 * WK);
    constexpr int PW = WK * WB / 4096;
    constexpr int NTN = N / BN, NTM = SEQ / BM;
    constexpr int RING = NSLOT * SLOT;
    constexpr int LDS_BYTES = RING + 2048;
    static_assert(WK * XB == 16384, "x pieces per slot must be 16");
    __shared__ __attribute__((aligned(16))) char lds[LDS_BYTES];

    const int tid = threadIdx.x, lane = tid & 63;
    const int wid = __builtin_amdgcn_readfirstlane(tid >> 6);
    int tm, tn;
    {
        const int x = blockIdx.x & 7, i = blockIdx.x >> 3;
        constexpr int XM = NTM / TMX;
        constexpr int XN = 8 / XM;
        constexpr int TNX = NTN / XN;
        tm = (x / XN) * TMX + (i % TMX);
        tn = (x % XN) * TNX + (i / TMX);
    }
    const int row0 = tm * BM, n0 = tn * BN;
    const unsigned lds0 = (unsigned)(uintptr_t)(lds_ptr)lds;

    if (__builtin_expect(wid < 4, 1)) {
        constexpr int PER = 4 + PW;
        const int l = wid;
        const int xr = 8 * l + (lane >> 3);
        const unsigned xoff = (unsigned)(xr * xp * 2 + (((lane & 7) ^ ((xr >> 1) & 7)) << 4));
        constexpr int RPP = 1024 / PB;
        const int wr = RPP * l + lane / (PB / 16), wc = lane % (PB / 16);
        const unsigned woff = (unsigned)(wr * WP * 2 + (((wc >> 1) ^ wswz<PB>(wr)) << 5) + ((wc & 1) << 4));
        constexpr int WSTEP = (BN == 96) ? 64 : 4 * RPP * WP * 2;
        const char* xg = (const char*)(X + (size_t)row0 * xp);
        const char* wg = (const char*)(LN ? W + n0 : W + (size_t)tn * (KT * BN));
        const int xstep = 64 * xp;
        auto issue = [&](int c) {
            const unsigned sb = lds0 + (c % NSLOT) * SLOT + l * 1024;
            const char* xs = xg + (size_t)c * (WK * 128);
            const char* ws = wg + (size_t)c * (WK * 64 * WP * 2);
#pragma unroll
            for (int i = 0; i < 4; ++i)
                glds16(xoff, xs + ((WK == 1) ? i * xstep : (i & 1) * xstep + (i >> 1) * 128), sb + i * 4096);
#pragma unroll
            for (int i = 0; i < PW; ++i) glds16(woff, ws + i * WSTEP, sb + 16384 + i * 4096);
        };
#pragma unroll
        for (int c = 0; c < LD_INIT; ++c) issue(c);
#pragma unroll
        for (int b = -1; b < NC - 1; ++b) {
            constexpr auto nis = [](int bb) { int n = LD_INIT; for (int t = -1; t < bb; ++t) { int cap = t + NSLOT + 1; n = (n + 1 < cap) ? n + 1 : cap; if (n > NC) n = NC; } return n; };
            const int before = nis(b), after = nis(b + 1);
            const int rem = before - (b + 2);
            if (rem == 0) wait_vm_bar<0>(); else if (rem == 1) wait_vm_bar<PER>(); else if (rem == 2) wait_vm_bar<2 * PER>();
            else if (rem == 3) wait_vm_bar<3 * PER>(); else wait_vm_bar<4 * PER>();
#pragma unroll
            for (int c = 0; c < NC; ++c) if (c >= before && c < after) issue(c);
        }
        return;
    }

    const int cw = wid - 4;
    const int r = lane & 15, g = lane >> 4;
    const int wk = (WK == 1) ? 0 : (cw >> 2);
    const int wm = (WK == 1) ? (cw >> 1) : ((cw >> 1) & 1);
    const int wn = cw & 1;
    const int mrot = LN ? wn : wk;
    const int jrot = LN ? (wm % TN) : 0;
    int jb[TN];
#pragma unroll
    for (int j = 0; j < TN; ++j) jb[j] = (j + jrot) % TN;
    const int mytok = row0 + wm * 32 + mrot * 16 + r;

    f32x4 pst[2];
    u32x2 auxv[TN];
    if (LN && !SELF) {
        const GAS f32x4* sp = (const GAS f32x4*)(stats + (size_t)mytok * 32 + g * 8);
        pst[0] = sp[0]; pst[1] = sp[1];
    }
    f32x4 csv[TN];
    if (LN && CSIN) {
#pragma unroll
        for (int j = 0; j < TN; ++j) csv[j] = *(const GAS f32x4*)(csg + n0 + wn * (BN / 2) + jb[j] * 16 + g * 4);
    }
    if (EPI == EPI_FFN1) {
#pragma unroll
        for (int j = 0; j < TN; ++j) auxv[j] = *(const GAS u32x2*)(aux + n0 + wn * (BN / 2) + jb[j] * 16 + g * 4);
    }
    if (EPI == EPI_RES) {
#pragma unroll
        for (int j = 0; j < 2; ++j) auxv[j] = *(const GAS u32x2*)(aux + (size_t)mytok * rp + n0 + wn * 32 + j * 16 + g * 4);
    }

    const int xrow = wm * 32 + r;
    const int xsw = (xrow >> 1) & 7;
    constexpr int NG = (NSLOT + 1) / 2;
    unsigned xa[NG][2][2], wa[NG][TN];
    {
        const int q = (lane >> 2) & 3, p = lane & 3;
        const int wrow = 8 * g + q;
#pragma unroll
        for (int gi = 0; gi < NG; ++gi) {
#pragma unroll
            for (int s = 0; s < 2; ++s)
#pragma unroll
                for (int m = 0; m < 2; ++m) {
                    xa[gi][s][m] = lds0 + gi * 2 * SLOT + wk * XB + (xrow + ((m ^ mrot) << 4)) * 128 + (((4 * s + g) ^ xsw) << 4);
                    asm volatile("" : "+v"(xa[gi][s][m]));
                }
#pragma unroll
            for (int j = 0; j < TN; ++j) {
                const int c16 = wn * TN + jb[j];
                wa[gi][j] = lds0 + gi * 2 * SLOT + WK * XB + wk * WB + (c16 / SPR) * (64 * PB) + wrow * PB +
                            (((c16 % SPR) ^ wswz<PB>(wrow)) << 5) + p * 8;
                asm volatile("" : "+v"(wa[gi][j]));
            }
        }
    }

    float* st = (float*)(lds + RING);
    float* cs = (float*)(lds + RING + 1024);
    auto put_stats = [&](float s1, float sq) {
        const float mu = s1 * (1.0f / KT);
        const float var = fmaxf(sq * (1.0f / KT) - mu * mu, 0.f);
        const float rs = __builtin_amdgcn_rsqf(var + 1e-5f);
        if (g == (r >> 2)) *(f32x2*)(st + (wm * 32 + mrot * 16 + r) * 2) = f32x2{mu * rs, rs};
    };

    f32x4 acc[2][TN];
#pragma unroll
    for (int m = 0; m < 2; ++m)
#pragma unroll
        for (int j = 0; j < TN; ++j) acc[m][j] = f32x4{0.f, 0.f, 0.f, 0.f};
    f32x4 xs1 = {0.f, 0.f, 0.f, 0.f}, xs2 = {0.f, 0.f, 0.f, 0.f}, wcs = {0.f, 0.f, 0.f, 0.f};
    const bf16x8 ones = {0x3f80, 0x3f80, 0x3f80, 0x3f80, 0x3f80, 0x3f80, 0x3f80, 0x3f80};

    bf16x8 xf[2][2][2], wf[2][2][TN];
    auto readf = [&](int c) {
        const int gi = (c % NSLOT) / 2, so = ((c % NSLOT) & 1) * SLOT, b = c & 1;
#pragma unroll
        for (int s = 0; s < 2; ++s) {
#pragma unroll
            for (int m = 0; m < 2; ++m) xf[b][s][m] = lds_b128((lds_cptr)(uintptr_t)xa[gi][s][m] + so);
#pragma unroll
            for (int j = 0; j < TN; ++j) {
                const s16x4 lo = lds_tr((lds_cptr)(uintptr_t)wa[gi][j] + so + (32 * s) * PB);
                const s16x4 hi = lds_tr((lds_cptr)(uintptr_t)wa[gi][j] + so + (32 * s + 4) * PB);
                wf[b][s][j] = __builtin_shufflevector(lo, hi, 0, 1, 2, 3, 4, 5, 6, 7);
            }
        }
    };
    auto fold_mfma = [&](int b) {
#pragma unroll
        for (int s = 0; s < 2; ++s) {
            if (LN && SELF) {
                xs1 = MFMA16(ones, xf[b][s][0], xs1);
                xs2 = MFMA16(xf[b][s][0], xf[b][s][0], xs2);
            }
            if (LN && !CSIN) wcs = MFMA16(wf[b][s][0], ones, wcs);
        }
    };
    auto main_mfma = [&](int b) {
#pragma unroll
        for (int s = 0; s < 2; ++s)
#pragma unroll
            for (int m = 0; m < 2; ++m)
#pragma unroll
                for (int j = 0; j < TN; ++j) acc[m][j] = MFMA16(wf[b][s][j], xf[b][s][m], acc[m][j]);
    };
    bar_lds();
    readf(0);
#pragma unroll
    for (int c = 0; c < NC - 1; ++c) {
        bar_lds();
        __builtin_amdgcn_sched_barrier(0);
        readf(c + 1);
        main_mfma(c & 1);
        fold_mfma(c & 1);
        constexpr int NM = 2 * (2 * TN + ((LN && SELF) ? 2 : 0) + ((LN && !CSIN) ? 1 : 0)), NR = 2 * (2 + 2 * TN);
        constexpr int RPM = (WK == 1) ? 2 : 3;
#pragma unroll
        for (int i = 0; i < NM; ++i) {
            __builtin_amdgcn_sched_group_barrier(0x008, 1, 0);
            if (i * RPM < NR) __builtin_amdgcn_sched_group_barrier(0x100, RPM, 0);
        }
        __builtin_amdgcn_sched_barrier(0);
        if (LN && !SELF && c == NC - 3) {
            float s1 = (pst[0][0] + pst[0][2]) + (pst[1][0] + pst[1][2]);
            float sq = (pst[0][1] + pst[0][3]) + (pst[1][1] + pst[1][3]);
            put_stats(sum_rows(s1), sum_rows(sq));
            __builtin_amdgcn_sched_barrier(0);
        }
    }
    fold_mfma((NC - 1) & 1);
    __builtin_amdgcn_sched_barrier(0);
    main_mfma((NC - 1) & 1);
    if (LN) {
        if (SELF) put_stats(xs1[0], (r & 2) ? ((r & 1) ? xs2[3] : xs2[2]) : ((r & 1) ? xs2[1] : xs2[0]));
        if (!CSIN && r == 0 && wm < TN) {
            *(f32x4*)(cs + wn * (BN / 2) + jrot * 16 + g * 4) = wcs;
            if (tm == 0) *(GAS f32x4*)(csg + n0 + wn * (BN / 2) + jrot * 16 + g * 4) = wcs;
        }
    }

    if (LN) {
        if (SELF || !CSIN) bar_lds();
        const unsigned stok = row0 + wm * 32 + (((g & 1) ^ mrot) << 4) + r;
        f32x2 ms[2];
#pragma unroll
        for (int m = 0; m < 2; ++m) ms[m] = *(const f32x2*)(st + (wm * 32 + ((m ^ mrot) << 4) + r) * 2);
#pragma unroll
        for (int j = 0; j < TN; ++j) {
            const int nl = wn * (BN / 2) + jb[j] * 16 + g * 4;
            const f32x4 c4 = CSIN ? csv[j] : *(const f32x4*)(cs + nl);
            if (EPI == EPI_QKV) {
                const int blk = (n0 + wn * (BN / 2)) / 16 + jb[j];
                const int sec = blk >> 5, head = blk & 31;
                const float sc = sec == 0 ? 0.25f * 1.4426950408889634f : 1.0f;
                char* base = (char*)out + (size_t)sec * (SEQ * DM * 2) + head * 65536;
                u32x2 o[2];
#pragma unroll
                for (int m = 0; m < 2; ++m) {
                    const float a = ms[m][1] * sc, bq = ms[m][0] * sc;
                    float v[4];
#pragma unroll
                    for (int e = 0; e < 4; ++e) v[e] = acc[m][j][e] * a - bq * c4[e];
                    o[m] = u32x2{pack_bf16(v[0], v[1]), pack_bf16(v[2], v[3])};
                }
                const unsigned off = sec < 2 ? (stok >> 5) * 1024 + (g >> 1) * 512 + (stok & 31) * 16 : stok * 32 + (g >> 1) * 16;
                store16_wt(base, off, widen16(o[0], o[1]));
            } else {
                constexpr float L2E = 1.4426950408889634f;
                const float bb[4] = {bf_lo(auxv[j][0]) * L2E, bf_hi(auxv[j][0]) * L2E, bf_lo(auxv[j][1]) * L2E, bf_hi(auxv[j][1]) * L2E};
                u32x2 o[2];
#pragma unroll
                for (int m = 0; m < 2; ++m) {
                    const float a = ms[m][1] * L2E, bq = ms[m][0] * L2E;
#pragma unroll
                    for (int e = 0; e < 4; e += 2) {
                        const f32x2 u = f32x2{acc[m][j][e], acc[m][j][e + 1]} * a + (f32x2{bb[e], bb[e + 1]} - f32x2{c4[e], c4[e + 1]} * bq);
                        const f32x2 ex = {__builtin_amdgcn_exp2f(u[0]), __builtin_amdgcn_exp2f(u[1])};
                        const f32x2 d = ex * (ex + 2.f) + 2.f;
                        const f32x2 rc = {__builtin_amdgcn_rcpf(d[0]), __builtin_amdgcn_rcpf(d[1])};
                        const f32x2 v = (u * rc) * -2.f + u;
                        o[m][e >> 1] = pack_bf16(v[0], v[1]);
                    }
                }
                store16_wt(out + n0 + wn * (BN / 2) + jb[j] * 16, (unsigned)(stok * N + (g >> 1) * 8) * 2, widen16(o[0], o[1]));
            }
        }
    } else {
        f32x4* ex = (f32x4*)(lds + ((NC - 2) % NSLOT) * SLOT);
#pragma unroll
        for (int j = 0; j < 2; ++j) ex[(cw * 2 + j) * 64 + lane] = acc[1][j];
        bar_lds();
        float s1 = 0.f, s2 = 0.f;
        u32x2 ov[2];
#pragma unroll
        for (int j = 0; j < 2; ++j) {
            const f32x4 o4 = ex[((cw ^ 4) * 2 + j) * 64 + lane];
            const f32x4 mine = acc[0][j];
            const float v0 = fmaf(mine[0] + o4[0], oscale, bf_lo(auxv[j][0])), v1 = fmaf(mine[1] + o4[1], oscale, bf_hi(auxv[j][0]));
            const float v2 = fmaf(mine[2] + o4[2], oscale, bf_lo(auxv[j][1])), v3 = fmaf(mine[3] + o4[3], oscale, bf_hi(auxv[j][1]));
            const u32x2 o = {pack_bf16(v0, v1), pack_bf16(v2, v3)};
            ov[j] = o;
            const float q0 = bf_lo(o[0]), q1 = bf_hi(o[0]), q2 = bf_lo(o[1]), q3 = bf_hi(o[1]);
            s1 += (q0 + q1) + (q2 + q3);
            s2 = fmaf(q0, q0, fmaf(q1, q1, fmaf(q2, q2, fmaf(q3, q3, s2))));
        }
        store16_wt(out + n0 + wn * 32, (unsigned)(mytok * op + (g & 1) * 16 + (g >> 1) * 8) * 2, widen16(ov[0], ov[1]));
        if (!SELF) {
            s1 = sum_rows(s1); s2 = sum_rows(s2);
            if (g == 0) *(GAS f32x2*)(stats + (size_t)mytok * 32 + (tn * 2 + wn) * 2) = f32x2{s1, s2};
        }
    }
}

constexpr int A_K = 0, A_V = 65536, A_ONE = 131072, A_Q = A_ONE + 8192, A_BYTES = A_Q + 8192;

DI float max16(const f32x16& s) {
    float a = fmaxf(fmaxf(s[0], s[1]), s[2]), b = fmaxf(fmaxf(s[3], s[4]), s[5]);
    a = fmaxf(fmaxf(a, s[6]), s[7]); b = fmaxf(fmaxf(b, s[8]), s[9]);
    a = fmaxf(fmaxf(a, s[10]), s[11]); b = fmaxf(fmaxf(b, s[12]), s[13]);
    a = fmaxf(fmaxf(a, s[14]), s[15]);
    a = fmaxf(a, b);
    auto rr = __builtin_amdgcn_permlane32_swap(__float_as_uint(a), __float_as_uint(a), false, false);
    return fmaxf(__uint_as_float(rr[0]), __uint_as_float(rr[1]));
}

#define A_EXP(S, i) "v_exp_f32 v[" #S "+" #i "], v[" #S "+" #i "]\n\t"
#define A_CVT(P, S, i) "v_cvt_pk_bf16_f32 v[" #P "], v[" #S "+" #i "], v[" #S "+" #i "+1]\n\t"
#define A_TILE(SC, SN, U)                                                                              \
    "s_waitcnt lgkmcnt(4)\n\t"                                                                         \
    "v_mfma_f32_32x32x16_bf16 v[" #SN ":" #SN "+15], v[136:139], %[q], %[nm]\n\t"                      \
    "ds_read_b128 v[136:139], %[ka] offset:(" #U "+2)*1024\n\t"                                        \
    A_EXP(SC, 0) A_EXP(SC, 1) A_EXP(SC, 2) A_EXP(SC, 3) A_EXP(SC, 4) A_EXP(SC, 5) A_EXP(SC, 6) A_EXP(SC, 7) \
    A_CVT(128, SC, 0) A_CVT(129, SC, 2) A_CVT(130, SC, 4) A_CVT(131, SC, 6)                            \
    A_EXP(SC, 8) A_EXP(SC, 9)                                                                          \
    "s_waitcnt lgkmcnt(3)\n\t"                                                                         \
    "v_mfma_f32_32x32x16_bf16 %[o], v[140:143], v[128:131], %[o]\n\t"                                  \
    "ds_read_b64_tr_b16 v[140:141], %[va] offset:(" #U "+1)*1024\n\t"                                  \
    "ds_read_b64_tr_b16 v[142:143], %[va] offset:(" #U "+1)*1024+256\n\t"                              \
    A_EXP(SC, 10) A_EXP(SC, 11) A_EXP(SC, 12) A_EXP(SC, 13) A_EXP(SC, 14) A_EXP(SC, 15)                \
    A_CVT(132, SC, 8) A_CVT(133, SC, 10) A_CVT(134, SC, 12) A_CVT(135, SC, 14)                         \
    "s_waitcnt lgkmcnt(3)\n\t"                                                                         \
    "s_nop 0\n\t"                                                                                      \
    "v_mfma_f32_32x32x16_bf16 %[o], v[144:147], v[132:135], %[o]\n\t"                                  \
    "ds_read_b64_tr_b16 v[144:145], %[va] offset:(" #U "+1)*1024+512\n\t"                              \
    "ds_read_b64_tr_b16 v[146:147], %[va] offset:(" #U "+1)*1024+768\n\t"

template <int ROWS, int CH> DI void warm_half(const uint16_t* w, int half, int t, unsigned lds_dummy) {
    static_assert((ROWS * CH) % 4096 == 0, "whole rounds only");
    const char* base = (const char*)w + half * (CH * 16);
#pragma unroll 1
    for (int k = 0; k < ROWS * CH / 4096; ++k) {
        const int idx = t + k * 4096;
        glds16((unsigned)(idx / CH) * (CH * 32) + (unsigned)(idx % CH) * 16, base, lds_dummy);
    }
}

template <int ROWS> DI void retile(const uint16_t* __restrict__ w, uint16_t* __restrict__ wt, int gt) {
#pragma unroll 1
    for (int idx = gt; idx < ROWS * 64; idx += 32768) {
        const int k = idx >> 6, c = idx & 63;
        *(u32x4*)(wt + ((size_t)(c >> 3) * ROWS + k) * 64 + (c & 7) * 8) = *(const u32x4*)(w + (size_t)k * 512 + c * 8);
    }
}

template <bool FIRST>
__global__ __launch_bounds__(640) void attn_kernel(const uint16_t* QKV, uint16_t* O, const uint16_t* wo, const uint16_t* w1,
                                                   const uint16_t* w2, uint16_t* wot, uint16_t* w2t) {
    asm volatile("" : "+s"(QKV), "+s"(O));
    __shared__ __attribute__((aligned(16))) char lds[A_BYTES];
    const int tid = threadIdx.x, lane = tid & 63;
    const int wid = __builtin_amdgcn_readfirstlane(tid >> 6);
    const int x = blockIdx.x & 7, i = blockIdx.x >> 3;
    const int head = x * 4 + (i >> 3), qb = i & 7;
    const int r = lane & 31, h = lane >> 5;
    const unsigned lds0 = (unsigned)(uintptr_t)(lds_ptr)lds;
    const char* Qg = (const char*)QKV + head * 65536;
    const unsigned lo = lane * 16;
    if (__builtin_expect(wid >= 8, 0)) {
        const char* kv = Qg + (size_t)(wid - 7) * (SEQ * DM * 2);
        const unsigned dst = lds0 + (wid - 8) * 65536;
        asm volatile("s_barrier" ::: "memory");
#pragma unroll 4
        for (int p = 12; p < 64; ++p) glds16(lo, kv + p * 1024, dst + p * 1024);
        asm volatile("s_waitcnt vmcnt(0)\n\ts_barrier" ::: "memory");
        const int t = (i * 2 + (wid - 8)) * 64 + lane;
        const unsigned dummy = lds0 + A_Q + (wid - 8) * 1024;
        if (FIRST) {
            retile<512>(wo, wot, x * 4096 + t);
            retile<1792>(w2, w2t, x * 4096 + t);
            warm_half<512, 112>(w1, x & 1, t, dummy);
        } else {
            warm_half<1, 16384>(wot, x & 1, t, dummy);
            warm_half<512, 112>(w1, x & 1, t, dummy);
            warm_half<1, 57344>(w2t, x & 1, t, dummy);
        }
        asm volatile("s_waitcnt vmcnt(0)" ::: "memory");
        return;
    }
    {
        const char* kv = Qg + (size_t)(1 + (wid & 1)) * (SEQ * DM * 2) + (wid >> 1) * 1024;
        const unsigned dst = lds0 + (wid & 1) * 65536 + (wid >> 1) * 1024;
        glds16(lo, Qg + (qb * 8 + wid) * 1024, lds0 + A_Q + wid * 1024);
#pragma unroll
        for (int p = 0; p < 3; ++p) glds16(lo, kv + p * 4096, dst + p * 4096);
    }
    *(u32x4*)(lds + A_ONE + tid * 16) = u32x4{(tid & 7) == 0 ? 0x00003f80u : 0u, 0u, 0u, 0u};

    const bool isv = (lane & 16) == 0;
    const unsigned ka0 = lds0 + A_K + h * 512 + r * 16;
    const unsigned va0 = isv ? lds0 + A_V + (4 * h + ((lane >> 2) & 3)) * 32 + (lane & 3) * 8
                             : lds0 + A_ONE + (1 - h) * 128 + ((lane & 3) ? 8 : 0);

    wait_vm_bar<0>();
    const bf16x8 qf = lds_b128((lds_cptr)lds + A_Q + wid * 1024 + h * 512 + r * 16);
    f32x16 negm;
    {
        f32x16 z = {};
        const f32x16 s0 = MFMA32(lds_b128((lds_cptr)(uintptr_t)ka0), qf, z);
        const float m0 = -max16(s0);
#pragma unroll
        for (int e = 0; e < 16; ++e) negm[e] = m0;
    }
    f32x16 o = {};
    {
        unsigned ka = ka0, va = va0;
        const unsigned vstep = isv ? 4096u : 0u;
        int it;
        asm volatile(
            "ds_read_b128 v[136:139], %[ka]\n\t"
            "s_mov_b32 %[it], 0\n\t"
            "s_waitcnt lgkmcnt(0)\n\t"
            "v_mfma_f32_32x32x16_bf16 v[96:111], v[136:139], %[q], %[nm]\n\t"
            "ds_read_b128 v[136:139], %[ka] offset:1024\n\t"
            "ds_read_b64_tr_b16 v[140:141], %[va]\n\t"
            "ds_read_b64_tr_b16 v[142:143], %[va] offset:256\n\t"
            "ds_read_b64_tr_b16 v[144:145], %[va] offset:512\n\t"
            "ds_read_b64_tr_b16 v[146:147], %[va] offset:768\n\t"
            "s_nop 7\n\t"
            "1:\n\t"
            A_TILE(96, 112, 0) A_TILE(112, 96, 1) A_TILE(96, 112, 2) A_TILE(112, 96, 3)
            "v_add_u32 %[ka], 0x1000, %[ka]\n\t"
            "v_add_u32 %[va], %[vs], %[va]\n\t"
            "s_add_u32 %[it], %[it], 1\n\t"
            "s_cmp_lg_u32 %[it], 2\n\t"
            "s_cbranch_scc1 2f\n\t"
            "s_barrier\n\t"
            "2:\n\t"
            "s_cmp_lg_u32 %[it], 16\n\t"
            "s_cbranch_scc1 1b\n\t"
            "s_waitcnt lgkmcnt(0)\n\t"
            "s_nop 15\n\t"
            : [o] "+v"(o), [ka] "+v"(ka), [va] "+v"(va), [it] "=&s"(it)
            : [q] "v"(qf), [nm] "v"(negm), [vs] "v"(vstep)
            : "memory", "scc", "v96", "v97", "v98", "v99", "v100", "v101", "v102", "v103", "v104", "v105", "v106", "v107",
              "v108", "v109", "v110", "v111", "v112", "v113", "v114", "v115", "v116", "v117", "v118", "v119", "v120", "v121",
              "v122", "v123", "v124", "v125", "v126", "v127", "v128", "v129", "v130", "v131", "v132", "v133", "v134", "v135",
              "v136", "v137", "v138", "v139", "v140", "v141", "v142", "v143", "v144", "v145", "v146", "v147");
    }

    bool bad = false;
#pragma unroll
    for (int e = 0; e < 9; ++e) bad |= !(__builtin_fabsf(o[e]) < __builtin_inff());
    if (__any(bad)) {
        lds_cptr k2 = (lds_cptr)(uintptr_t)ka0;
        lds_cptr v2 = (lds_cptr)(uintptr_t)va0;
        const int vs2 = isv ? 1024 : 0;
        float m = -__builtin_inff();
        o = f32x16{};
#pragma unroll 1
        for (int t = 0; t < 64; ++t) {
            f32x16 z = {};
            const f32x16 s = MFMA32(lds_b128(k2), qf, z);
            const float mn = fmaxf(m, max16(s));
            const float al = __builtin_amdgcn_exp2f(m - mn);
            m = mn;
#pragma unroll
            for (int e = 0; e < 16; ++e) o[e] *= al;
            u32x4 p0, p1;
#pragma unroll
            for (int e = 0; e < 4; ++e) {
                p0[e] = pack_bf16(__builtin_amdgcn_exp2f(s[2 * e] - mn), __builtin_amdgcn_exp2f(s[2 * e + 1] - mn));
                p1[e] = pack_bf16(__builtin_amdgcn_exp2f(s[8 + 2 * e] - mn), __builtin_amdgcn_exp2f(s[8 + 2 * e + 1] - mn));
            }
            const s16x4 v00 = lds_tr(v2), v01 = lds_tr(v2 + 256), v10 = lds_tr(v2 + 512), v11 = lds_tr(v2 + 768);
            o = MFMA32(__builtin_shufflevector(v00, v01, 0, 1, 2, 3, 4, 5, 6, 7), __builtin_bit_cast(bf16x8, p0), o);
            o = MFMA32(__builtin_shufflevector(v10, v11, 0, 1, 2, 3, 4, 5, 6, 7), __builtin_bit_cast(bf16x8, p1), o);
            k2 += 1024;
            v2 += vs2;
        }
    }
    auto lsw = __builtin_amdgcn_permlane32_swap(__float_as_uint(o[8]), __float_as_uint(o[8]), false, false);
    const float rl = 1.0f / __uint_as_float(lsw[0]);
    auto sx = __builtin_amdgcn_permlane32_swap(pack_bf16(o[0] * rl, o[1] * rl), pack_bf16(o[4] * rl, o[5] * rl), false, false);
    auto sy = __builtin_amdgcn_permlane32_swap(pack_bf16(o[2] * rl, o[3] * rl), pack_bf16(o[6] * rl, o[7] * rl), false, false);
    store16_wt(O + head * 16, (unsigned)((qb * 256 + wid * 32 + r) * LDA + h * 8) * 2, u32x4{sx[0], sy[0], sx[1], sy[1]});
}

extern "C" void kernel_launch(void* const* d_in, const int* in_sizes, int n_in, void* d_out, int out_size, void* d_ws,
                              size_t ws_size, hipStream_t stream) {
    const uint16_t* x0 = (const uint16_t*)d_in[0];
    const uint16_t* wqkv = (const uint16_t*)d_in[1];
    const uint16_t* wo = (const uint16_t*)d_in[2];
    const uint16_t* w1 = (const uint16_t*)d_in[4];
    const uint16_t* b1 = (const uint16_t*)d_in[5];
    const uint16_t* w2 = (const uint16_t*)d_in[6];
    char* ws = (char*)d_ws;
    constexpr size_t MB = 1u << 20;
    uint16_t* qkv = (uint16_t*)ws;
    uint16_t* ob = (uint16_t*)(ws + 6 * MB);
    uint16_t* x1 = (uint16_t*)(ws + 9 * MB);
    uint16_t* x2 = (uint16_t*)(ws + 12 * MB);
    uint16_t* hb = (uint16_t*)(ws + 15 * MB);
    float* st1 = (float*)(ws + 23 * MB);
    float* st2 = (float*)(ws + 24 * MB);
    uint16_t* wot = (uint16_t*)(ws + 25 * MB);
    uint16_t* w2t = (uint16_t*)(ws + 26 * MB);
    float* csq = (float*)(ws + 28 * MB);
    float* cs1 = (float*)(ws + 28 * MB + 65536);
    uint16_t* out = (uint16_t*)d_out;
    constexpr float LN2 = 0.6931471805599453f;
    gemm_kernel<EPI_QKV, 128, 96, 512, 1536, 1, 5, 4, true, false><<<256, 768, 0, stream>>>(x0, wqkv, nullptr, qkv, nullptr, csq, 1.0f, DM, 0, 0);
    attn_kernel<true><<<256, 640, 0, stream>>>(qkv, ob, wo, w1, w2, wot, w2t);
    gemm_kernel<EPI_RES, 64, 64, 512, 512, 2, 4, 8, false, false><<<256, 768, 0, stream>>>(ob, wot, x0, x1, st1, nullptr, 1.0f, LDA, DM, LDA);
    gemm_kernel<EPI_FFN1, 128, 128, 512, 1792, 1, 4, 4, false, false><<<224, 768, 0, stream>>>(x1, w1, b1, hb, st1, cs1, 1.0f, LDA, 0, 0);
    gemm_kernel<EPI_RES, 64, 64, 1792, 512, 2, 4, 8, false, false><<<256, 768, 0, stream>>>(hb, w2t, x1, x2, st2, nullptr, LN2, FF, LDA, LDA);
    gemm_kernel<EPI_QKV, 128, 96, 512, 1536, 1, 5, 4, false, true><<<256, 768, 0, stream>>>(x2, wqkv, nullptr, qkv, st2, csq, 1.0f, LDA, 0, 0);
    attn_kernel<false><<<256, 640, 0, stream>>>(qkv, ob, wo, w1, w2, wot, w2t);
    gemm_kernel<EPI_RES, 64, 64, 512, 512, 2, 4, 8, false, false><<<256, 768, 0, stream>>>(ob, wot, x2, x1, st1, nullptr, 1.0f, LDA, LDA, LDA);
    gemm_kernel<EPI_FFN1, 128, 128, 512, 1792, 1, 4, 4, false, true><<<224, 768, 0, stream>>>(x1, w1, b1, hb, st1, cs1, 1.0f, LDA, 0, 0);
    gemm_kernel<EPI_RES, 64, 64, 1792, 512, 2, 4, 8, true, false><<<256, 768, 0, stream>>>(hb, w2t, x1, out, st2, nullptr, LN2, FF, LDA, DM);
}
```

```cpp
#include <hip/hip_runtime.h>
#include <hip/hip_bf16.h>
#include <stdint.h>

typedef __attribute__((ext_vector_type(8))) short bf16x8;
typedef __attribute__((ext_vector_type(4))) short s16x4;
typedef __attribute__((ext_vector_type(4))) float f32x4;
typedef __attribute__((ext_vector_type(2))) float f32x2;
typedef __attribute__((ext_vector_type(16))) float f32x16;
typedef __attribute__((ext_vector_type(4))) unsigned u32x4;
typedef __attribute__((ext_vector_type(2))) unsigned u32x2;
typedef __attribute__((ext_vector_type(2))) __bf16 bf16x2_t;
typedef __attribute__((address_space(3))) const char* lds_cptr;
typedef __attribute__((address_space(3))) char* lds_ptr;

#define DI __device__ __forceinline__
#define GAS __attribute__((address_space(1)))

constexpr int SEQ = 2048, DM = 512, NH = 32, FF = 1792;
constexpr int LDA = 576;

DI unsigned pack_bf16(float lo, float hi) {
    f32x2 v = {lo, hi};
    return __builtin_bit_cast(unsigned, __builtin_convertvector(v, bf16x2_t));
}
DI float bf_lo(unsigned u) { return __uint_as_float(u << 16); }
DI float bf_hi(unsigned u) { return __uint_as_float(u & 0xffff0000u); }
DI float dot2bf(unsigned a, unsigned b, float c) {
    return __builtin_amdgcn_fdot2_f32_bf16(__builtin_bit_cast(bf16x2_t, a), __builtin_bit_cast(bf16x2_t, b), c, false);
}
DI void glds16(unsigned voff, const void* sbase, unsigned lds_base) {
    asm volatile("s_mov_b32 m0, %2\n\ts_nop 0\n\tglobal_load_lds_dwordx4 %0, %1" ::"v"(voff), "s"(sbase), "s"(lds_base) : "memory");
}
DI void store16_wt(void* sbase, unsigned voff, u32x4 v) {
    asm volatile("global_store_dwordx4 %0, %1, %2 sc1\n\ts_nop 1" ::"v"(voff), "v"(v), "s"(sbase) : "memory");
}
DI u32x4 widen16(u32x2 a, u32x2 b) {
    auto x = __builtin_amdgcn_permlane16_swap(a[0], b[0], false, false);
    auto y = __builtin_amdgcn_permlane16_swap(a[1], b[1], false, false);
    return u32x4{x[0], y[0], x[1], y[1]};
}
DI float sum_rows(float v) {
    auto a = __builtin_amdgcn_permlane16_swap(__float_as_uint(v), __float_as_uint(v), false, false);
    v = __uint_as_float(a[0]) + __uint_as_float(a[1]);
    auto b = __builtin_amdgcn_permlane32_swap(__float_as_uint(v), __float_as_uint(v), false, false);
    return __uint_as_float(b[0]) + __uint_as_float(b[1]);
}
DI s16x4 lds_tr(lds_cptr p) {
    typedef short v4i16_t __attribute__((ext_vector_type(4)));
    return __builtin_bit_cast(s16x4, __builtin_amdgcn_ds_read_tr16_b64_v4i16((__attribute__((address_space(3))) v4i16_t*)p));
}
DI bf16x8 lds_b128(lds_cptr p) { return *(const __attribute__((address_space(3))) bf16x8*)p; }
template <int N> DI void wait_vm_bar() {
    __builtin_amdgcn_s_waitcnt(0xC07F);
    asm volatile("s_waitcnt vmcnt(%0) lgkmcnt(0)\n\ts_barrier" ::"i"(N) : "memory");
}

DI void bar_lds() {
    __builtin_amdgcn_s_waitcnt(0xC07F);
    asm volatile("s_waitcnt lgkmcnt(0)\n\ts_barrier" ::: "memory");
}

#define CODE_ADDR(sym) ({ unsigned long long a_;                                                                                  \
    asm volatile("s_getpc_b64 s[98:99]\n\ts_add_u32 s98, s98, " #sym "@rel32@lo+4\n\ts_addc_u32 s99, s99, " #sym "@rel32@hi+12\n\t" \
                 "s_mov_b64 %0, s[98:99]" : "=s"(a_) :: "scc", "s98", "s99"); (const char*)a_; })

#define MFMA16(a, b, c) __builtin_amdgcn_mfma_f32_16x16x32_bf16((a), (b), (c), 0, 0, 0)
#define MFMA32(a, b, c) __builtin_amdgcn_mfma_f32_32x32x16_bf16((a), (b), (c), 0, 0, 0)

enum { EPI_QKV = 0, EPI_FFN1 = 1, EPI_RES = 2 };
#define LD_INIT 2

template <int PB> DI int wswz(int row) {
    if (PB == 64) return (row >> 3) & 1;
    if (PB == 128) return ((row >> 1) & 1) | (((row >> 3) & 1) << 1);
    return (row & 3) | (((row >> 3) & 1) << 2);
}

template <int EPI, int BM, int BN, int KT, int N, int WK, int NSLOT, int TMX  , bool SELF, bool CSIN>
DI void gemm_body(const uint16_t* X, const uint16_t* W, const uint16_t* aux  , uint16_t* out,
                  float* stats, float* csg, float oscale, int xp, int rp, int op  ,
                  const char* next_code, int next_kb) {
    asm volatile("" : "+s"(X), "+s"(W), "+s"(aux), "+s"(out), "+s"(stats), "+s"(csg), "+s"(oscale), "+s"(xp), "+s"(rp), "+s"(op));
    constexpr bool LN = (EPI != EPI_RES);
    constexpr int WP = LN ? N : BN;
    constexpr int TN = (WK == 1) ? BN / 32 : 2;
    constexpr int PB = (BN == 96) ? 64 : BN * 2;
    constexpr int SPR = PB / 32;
    constexpr int XB = BM * 128, WB = 64 * BN * 2;
    constexpr int SLOT = WK * (XB + WB);
    constexpr int NC = KT / (64 * WK);
    constexpr int PW = WK * WB / 4096;
    constexpr int NTN = N / BN, NTM = SEQ / BM;
    constexpr int RING = NSLOT * SLOT;
    constexpr int LDS_BYTES = RING + 3072;
    static_assert(WK * XB == 16384, "x pieces per slot must be 16");
    __shared__ __attribute__((aligned(16))) char lds[LDS_BYTES];

    const int tid = threadIdx.x, lane = tid & 63;
    const int wid = __builtin_amdgcn_readfirstlane(tid >> 6);
    int tm, tn;
    {
        const int x = blockIdx.x & 7, i = blockIdx.x >> 3;
        constexpr int XM = NTM / TMX;
        constexpr int XN = 8 / XM;
        constexpr int TNX = NTN / XN;
        tm = (x / XN) * TMX + (i % TMX);
        tn = (x % XN) * TNX + (i / TMX);
    }
    const int row0 = tm * BM, n0 = tn * BN;
    const unsigned lds0 = (unsigned)(uintptr_t)(lds_ptr)lds;

    if (__builtin_expect(wid < 4, 1)) {
        constexpr int PER = 4 + PW;
        const int l = wid;
        const int xr = 8 * l + (lane >> 3);
        const unsigned xoff = (unsigned)(xr * xp * 2 + (((lane & 7) ^ ((xr >> 1) & 7)) << 4));
        constexpr int RPP = 1024 / PB;
        const int wr = RPP * l + lane / (PB / 16), wc = lane % (PB / 16);
        const unsigned woff = (unsigned)(wr * WP * 2 + (((wc >> 1) ^ wswz<PB>(wr)) << 5) + ((wc & 1) << 4));
        constexpr int WSTEP = (BN == 96) ? 64 : 4 * RPP * WP * 2;
        const char* xg = (const char*)(X + (size_t)row0 * xp);
        const char* wg = (const char*)(LN ? W + n0 : W + (size_t)tn * (KT * BN));
        const int xstep = 64 * xp;
        auto issue = [&](int c) {
            const unsigned sb = lds0 + (c % NSLOT) * SLOT + l * 1024;
            const char* xs = xg + (size_t)c * (WK * 128);
            const char* ws = wg + (size_t)c * (WK * 64 * WP * 2);
#pragma unroll
            for (int i = 0; i < 4; ++i)
                glds16(xoff, xs + ((WK == 1) ? i * xstep : (i & 1) * xstep + (i >> 1) * 128), sb + i * 4096);
#pragma unroll
            for (int i = 0; i < PW; ++i) glds16(woff, ws + i * WSTEP, sb + 16384 + i * 4096);
        };
#pragma unroll
        for (int c = 0; c < LD_INIT; ++c) issue(c);
#pragma unroll
        for (int b = -1; b < NC - 1; ++b) {
            constexpr auto nis = [](int bb) { int n = LD_INIT; for (int t = -1; t < bb; ++t) { int cap = t + NSLOT + 1; n = (n + 1 < cap) ? n + 1 : cap; if (n > NC) n = NC; } return n; };
            const int before = nis(b), after = nis(b + 1);
            const int rem = before - (b + 2);
            if (rem == 0) wait_vm_bar<0>(); else if (rem == 1) wait_vm_bar<PER>(); else if (rem == 2) wait_vm_bar<2 * PER>();
            else if (rem == 3) wait_vm_bar<3 * PER>(); else wait_vm_bar<4 * PER>();
#pragma unroll
            for (int c = 0; c < NC; ++c) if (c >= before && c < after) issue(c);
        }
        if (l == 0 && (int)(blockIdx.x >> 3) < next_kb) {
            glds16(lane * 16, next_code + (blockIdx.x >> 3) * 1024, lds0 + RING + 2048);
            asm volatile("s_waitcnt vmcnt(0)" ::: "memory");
        }
        return;
    }

    const int cw = wid - 4;
    const int r = lane & 15, g = lane >> 4;
    const int wk = (WK == 1) ? 0 : (cw >> 2);
    const int wm = (WK == 1) ? (cw >> 1) : ((cw >> 1) & 1);
    const int wn = cw & 1;
    const int mrot = LN ? wn : wk;
    const int jrot = LN ? (wm % TN) : 0;
    int jb[TN];
#pragma unroll
    for (int j = 0; j < TN; ++j) jb[j] = (j + jrot) % TN;
    const int mytok = row0 + wm * 32 + mrot * 16 + r;

    f32x4 pst[2];
    u32x2 auxv[TN];
    if (LN && !SELF) {
        const GAS f32x4* sp = (const GAS f32x4*)(stats + (size_t)mytok * 32 + g * 8);
        pst[0] = sp[0]; pst[1] = sp[1];
    }
    f32x4 csv[TN];
    if (LN && CSIN) {
#pragma unroll
        for (int j = 0; j < TN; ++j) csv[j] = *(const GAS f32x4*)(csg + n0 + wn * (BN / 2) + jb[j] * 16 + g * 4);
    }
    if (EPI == EPI_FFN1) {
#pragma unroll
        for (int j = 0; j < TN; ++j) auxv[j] = *(const GAS u32x2*)(aux + n0 + wn * (BN / 2) + jb[j] * 16 + g * 4);
    }
    if (EPI == EPI_RES) {
#pragma unroll
        for (int j = 0; j < 2; ++j) auxv[j] = *(const GAS u32x2*)(aux + (size_t)mytok * rp + n0 + wn * 32 + j * 16 + g * 4);
    }

    const int xrow = wm * 32 + r;
    const int xsw = (xrow >> 1) & 7;
    constexpr int NG = (NSLOT + 1) / 2;
    unsigned xa[NG][2][2], wa[NG][TN];
    {
        const int q = (lane >> 2) & 3, p = lane & 3;
        const int wrow = 8 * g + q;
#pragma unroll
        for (int gi = 0; gi < NG; ++gi) {
#pragma unroll
            for (int s = 0; s < 2; ++s)
#pragma unroll
                for (int m = 0; m < 2; ++m) {
                    xa[gi][s][m] = lds0 + gi * 2 * SLOT + wk * XB + (xrow + ((m ^ mrot) << 4)) * 128 + (((4 * s + g) ^ xsw) << 4);
                    asm volatile("" : "+v"(xa[gi][s][m]));
                }
#pragma unroll
            for (int j = 0; j < TN; ++j) {
                const int c16 = wn * TN + jb[j];
                wa[gi][j] = lds0 + gi * 2 * SLOT + WK * XB + wk * WB + (c16 / SPR) * (64 * PB) + wrow * PB +
                            (((c16 % SPR) ^ wswz<PB>(wrow)) << 5) + p * 8;
                asm volatile("" : "+v"(wa[gi][j]));
            }
        }
    }

    float* st = (float*)(lds + RING);
    float* cs = (float*)(lds + RING + 1024);
    auto put_stats = [&](float s1, float sq) {
        const float mu = s1 * (1.0f / KT);
        const float var = fmaxf(sq * (1.0f / KT) - mu * mu, 0.f);
        const float rs = __builtin_amdgcn_rsqf(var + 1e-5f);
        if (g == (r >> 2)) *(f32x2*)(st + (wm * 32 + mrot * 16 + r) * 2) = f32x2{mu * rs, rs};
    };

    f32x4 acc[2][TN];
#pragma unroll
    for (int m = 0; m < 2; ++m)
#pragma unroll
        for (int j = 0; j < TN; ++j) acc[m][j] = f32x4{0.f, 0.f, 0.f, 0.f};
    f32x4 xs1 = {0.f, 0.f, 0.f, 0.f}, xs2 = {0.f, 0.f, 0.f, 0.f}, wcs = {0.f, 0.f, 0.f, 0.f};
    const bf16x8 ones = {0x3f80, 0x3f80, 0x3f80, 0x3f80, 0x3f80, 0x3f80, 0x3f80, 0x3f80};

    bf16x8 xf[2][2][2], wf[2][2][TN];
    auto readf = [&](int c) {
        const int gi = (c % NSLOT) / 2, so = ((c % NSLOT) & 1) * SLOT, b = c & 1;
#pragma unroll
        for (int s = 0; s < 2; ++s) {
#pragma unroll
            for (int m = 0; m < 2; ++m) xf[b][s][m] = lds_b128((lds_cptr)(uintptr_t)xa[gi][s][m] + so);
#pragma unroll
            for (int j = 0; j < TN; ++j) {
                const s16x4 lo = lds_tr((lds_cptr)(uintptr_t)wa[gi][j] + so + (32 * s) * PB);
                const s16x4 hi = lds_tr((lds_cptr)(uintptr_t)wa[gi][j] + so + (32 * s + 4) * PB);
                wf[b][s][j] = __builtin_shufflevector(lo, hi, 0, 1, 2, 3, 4, 5, 6, 7);
            }
        }
    };
    auto fold_mfma = [&](int b) {
#pragma unroll
        for (int s = 0; s < 2; ++s) {
            if (LN && SELF) {
                xs1 = MFMA16(ones, xf[b][s][0], xs1);
                xs2 = MFMA16(xf[b][s][0], xf[b][s][0], xs2);
            }
            if (LN && !CSIN) wcs = MFMA16(wf[b][s][0], ones, wcs);
        }
    };
    auto main_mfma = [&](int b) {
#pragma unroll
        for (int s = 0; s < 2; ++s)
#pragma unroll
            for (int m = 0; m < 2; ++m)
#pragma unroll
                for (int j = 0; j < TN; ++j) acc[m][j] = MFMA16(wf[b][s][j], xf[b][s][m], acc[m][j]);
    };
    bar_lds();
    readf(0);
#pragma unroll
    for (int c = 0; c < NC - 1; ++c) {
        bar_lds();
        __builtin_amdgcn_sched_barrier(0);
        readf(c + 1);
        main_mfma(c & 1);
        fold_mfma(c & 1);
        constexpr int NM = 2 * (2 * TN + ((LN && SELF) ? 2 : 0) + ((LN && !CSIN) ? 1 : 0)), NR = 2 * (2 + 2 * TN);
        constexpr int RPM = (WK == 1) ? 2 : 3;
#pragma unroll
        for (int i = 0; i < NM; ++i) {
            __builtin_amdgcn_sched_group_barrier(0x008, 1, 0);
            if (i * RPM < NR) __builtin_amdgcn_sched_group_barrier(0x100, RPM, 0);
        }
        __builtin_amdgcn_sched_barrier(0);
        if (LN && !SELF && c == NC - 3) {
            float s1 = (pst[0][0] + pst[0][2]) + (pst[1][0] + pst[1][2]);
            float sq = (pst[0][1] + pst[0][3]) + (pst[1][1] + pst[1][3]);
            put_stats(sum_rows(s1), sum_rows(sq));
            __builtin_amdgcn_sched_barrier(0);
        }
    }
    fold_mfma((NC - 1) & 1);
    __builtin_amdgcn_sched_barrier(0);
    main_mfma((NC - 1) & 1);
    if (LN) {
        if (SELF) put_stats(xs1[0], (r & 2) ? ((r & 1) ? xs2[3] : xs2[2]) : ((r & 1) ? xs2[1] : xs2[0]));
        if (!CSIN && r == 0 && wm < TN) {
            *(f32x4*)(cs + wn * (BN / 2) + jrot * 16 + g * 4) = wcs;
            if (tm == 0) *(GAS f32x4*)(csg + n0 + wn * (BN / 2) + jrot * 16 + g * 4) = wcs;
        }
    }

    if (LN) {
        if (SELF || !CSIN) bar_lds();
        const unsigned stok = row0 + wm * 32 + (((g & 1) ^ mrot) << 4) + r;
        f32x2 ms[2];
#pragma unroll
        for (int m = 0; m < 2; ++m) ms[m] = *(const f32x2*)(st + (wm * 32 + ((m ^ mrot) << 4) + r) * 2);
#pragma unroll
        for (int j = 0; j < TN; ++j) {
            const int nl = wn * (BN / 2) + jb[j] * 16 + g * 4;
            const f32x4 c4 = CSIN ? csv[j] : *(const f32x4*)(cs + nl);
            if (EPI == EPI_QKV) {
                const int blk = (n0 + wn * (BN / 2)) / 16 + jb[j];
                const int sec = blk >> 5, head = blk & 31;
                const float sc = sec == 0 ? 0.25f * 1.4426950408889634f : 1.0f;
                char* base = (char*)out + (size_t)sec * (SEQ * DM * 2) + head * 65536;
                u32x2 o[2];
#pragma unroll
                for (int m = 0; m < 2; ++m) {
                    const float a = ms[m][1] * sc, bq = ms[m][0] * sc;
                    float v[4];
#pragma unroll
                    for (int e = 0; e < 4; ++e) v[e] = acc[m][j][e] * a - bq * c4[e];
                    o[m] = u32x2{pack_bf16(v[0], v[1]), pack_bf16(v[2], v[3])};
                }
                const unsigned off = sec < 2 ? (stok >> 5) * 1024 + (g >> 1) * 512 + (stok & 31) * 16 : stok * 32 + (g >> 1) * 16;
                store16_wt(base, off, widen16(o[0], o[1]));
            } else {
                constexpr float L2E = 1.4426950408889634f;
                const float bb[4] = {bf_lo(auxv[j][0]) * L2E, bf_hi(auxv[j][0]) * L2E, bf_lo(auxv[j][1]) * L2E, bf_hi(auxv[j][1]) * L2E};
                u32x2 o[2];
#pragma unroll
                for (int m = 0; m < 2; ++m) {
                    const float a = ms[m][1] * L2E, bq = ms[m][0] * L2E;
#pragma unroll
                    for (int e = 0; e < 4; e += 2) {
                        const f32x2 u = f32x2{acc[m][j][e], acc[m][j][e + 1]} * a + (f32x2{bb[e], bb[e + 1]} - f32x2{c4[e], c4[e + 1]} * bq);
                        const f32x2 ex = {__builtin_amdgcn_exp2f(u[0]), __builtin_amdgcn_exp2f(u[1])};
                        const f32x2 d = ex * (ex + 2.f) + 2.f;
                        const f32x2 rc = {__builtin_amdgcn_rcpf(d[0]), __builtin_amdgcn_rcpf(d[1])};
                        const f32x2 v = (u * rc) * -2.f + u;
                        o[m][e >> 1] = pack_bf16(v[0], v[1]);
                    }
                }
#pragma unroll
                for (int m = 0; m < 2; ++m)
                    *(u32x2*)(lds + cw * 4608 + ((((m ^ mrot) << 4) + r) * 144 + jb[j] * 32 + g * 8)) = o[m];
            }
        }
        if (EPI == EPI_FFN1) {
#pragma unroll
            for (int i = 0; i < 4; ++i) {
                const int row = i * 8 + (lane >> 3);
                const u32x4 v = *(const u32x4*)(lds + cw * 4608 + row * 144 + (lane & 7) * 16);
                store16_wt(out + n0 + wn * (BN / 2), (unsigned)((row0 + wm * 32 + row) * N + (lane & 7) * 8) * 2, v);
            }
        }
    } else {
        f32x4* ex = (f32x4*)(lds + ((NC - 2) % NSLOT) * SLOT);
#pragma unroll
        for (int j = 0; j < 2; ++j) ex[(cw * 2 + j) * 64 + lane] = acc[1][j];
        bar_lds();
        float s1 = 0.f, s2 = 0.f;
        u32x2 ov[2];
#pragma unroll
        for (int j = 0; j < 2; ++j) {
            const f32x4 o4 = ex[((cw ^ 4) * 2 + j) * 64 + lane];
            const f32x4 mine = acc[0][j];
            const float v0 = fmaf(mine[0] + o4[0], oscale, bf_lo(auxv[j][0])), v1 = fmaf(mine[1] + o4[1], oscale, bf_hi(auxv[j][0]));
            const float v2 = fmaf(mine[2] + o4[2], oscale, bf_lo(auxv[j][1])), v3 = fmaf(mine[3] + o4[3], oscale, bf_hi(auxv[j][1]));
            const u32x2 o = {pack_bf16(v0, v1), pack_bf16(v2, v3)};
            ov[j] = o;
            const float q0 = bf_lo(o[0]), q1 = bf_hi(o[0]), q2 = bf_lo(o[1]), q3 = bf_hi(o[1]);
            s1 += (q0 + q1) + (q2 + q3);
            s2 = fmaf(q0, q0, fmaf(q1, q1, fmaf(q2, q2, fmaf(q3, q3, s2))));
        }
        store16_wt(out + n0 + wn * 32, (unsigned)(mytok * op + (g & 1) * 16 + (g >> 1) * 8) * 2, widen16(ov[0], ov[1]));
        if (stats) {
            s1 = sum_rows(s1); s2 = sum_rows(s2);
            if (g == 0) *(GAS f32x2*)(stats + (size_t)mytok * 32 + (tn * 2 + wn) * 2) = f32x2{s1, s2};
        }
    }
}

constexpr int A_K = 0, A_V = 65536, A_ONE = 131072, A_Q = A_ONE + 8192, A_BYTES = A_Q + 8192;

DI float max16(const f32x16& s) {
    float a = fmaxf(fmaxf(s[0], s[1]), s[2]), b = fmaxf(fmaxf(s[3], s[4]), s[5]);
    a = fmaxf(fmaxf(a, s[6]), s[7]); b = fmaxf(fmaxf(b, s[8]), s[9]);
    a = fmaxf(fmaxf(a, s[10]), s[11]); b = fmaxf(fmaxf(b, s[12]), s[13]);
    a = fmaxf(fmaxf(a, s[14]), s[15]);
    a = fmaxf(a, b);
    auto rr = __builtin_amdgcn_permlane32_swap(__float_as_uint(a), __float_as_uint(a), false, false);
    return fmaxf(__uint_as_float(rr[0]), __uint_as_float(rr[1]));
}

#define A_EXP(S, i) "v_exp_f32 v[" #S "+" #i "], v[" #S "+" #i "]\n\t"
#define A_CVT(P, S, i) "v_cvt_pk_bf16_f32 v[" #P "], v[" #S "+" #i "], v[" #S "+" #i "+1]\n\t"
#define A_TILE(SC, SN, U)                                                                              \
    "s_waitcnt lgkmcnt(4)\n\t"                                                                         \
    "v_mfma_f32_32x32x16_bf16 v[" #SN ":" #SN "+15], v[136:139], %[q], %[nm]\n\t"                      \
    "ds_read_b128 v[136:139], %[ka] offset:(" #U "+2)*1024\n\t"                                        \
    A_EXP(SC, 0) A_EXP(SC, 1) A_EXP(SC, 2) A_EXP(SC, 3) A_EXP(SC, 4) A_EXP(SC, 5) A_EXP(SC, 6) A_EXP(SC, 7) \
    A_CVT(128, SC, 0) A_CVT(129, SC, 2) A_CVT(130, SC, 4) A_CVT(131, SC, 6)                            \
    A_EXP(SC, 8) A_EXP(SC, 9)                                                                          \
    "s_waitcnt lgkmcnt(3)\n\t"                                                                         \
    "v_mfma_f32_32x32x16_bf16 %[o], v[140:143], v[128:131], %[o]\n\t"                                  \
    "ds_read_b64_tr_b16 v[140:141], %[va] offset:(" #U "+1)*1024\n\t"                                  \
    "ds_read_b64_tr_b16 v[142:143], %[va] offset:(" #U "+1)*1024+256\n\t"                              \
    A_EXP(SC, 10) A_EXP(SC, 11) A_EXP(SC, 12) A_EXP(SC, 13) A_EXP(SC, 14) A_EXP(SC, 15)                \
    A_CVT(132, SC, 8) A_CVT(133, SC, 10) A_CVT(134, SC, 12) A_CVT(135, SC, 14)                         \
    "s_waitcnt lgkmcnt(3)\n\t"                                                                         \
    "s_nop 0\n\t"                                                                                      \
    "v_mfma_f32_32x32x16_bf16 %[o], v[144:147], v[132:135], %[o]\n\t"                                  \
    "ds_read_b64_tr_b16 v[144:145], %[va] offset:(" #U "+1)*1024+512\n\t"                              \
    "ds_read_b64_tr_b16 v[146:147], %[va] offset:(" #U "+1)*1024+768\n\t"

template <int ROWS, int CH> DI void warm_half(const uint16_t* w, int half, int t, unsigned lds_dummy) {
    static_assert((ROWS * CH) % 4096 == 0, "whole rounds only");
    const char* base = (const char*)w + half * (CH * 16);
#pragma unroll 1
    for (int k = 0; k < ROWS * CH / 4096; ++k) {
        const int idx = t + k * 4096;
        glds16((unsigned)(idx / CH) * (CH * 32) + (unsigned)(idx % CH) * 16, base, lds_dummy);
    }
}

template <int ROWS> DI void retile(const uint16_t* __restrict__ w, uint16_t* __restrict__ wt, int gt) {
#pragma unroll 1
    for (int idx = gt; idx < ROWS * 64; idx += 32768) {
        const int k = idx >> 6, c = idx & 63;
        *(u32x4*)(wt + ((size_t)(c >> 3) * ROWS + k) * 64 + (c & 7) * 8) = *(const u32x4*)(w + (size_t)k * 512 + c * 8);
    }
}

extern "C" __global__ void k_oproj(const uint16_t*, const uint16_t*, const uint16_t*, uint16_t*, float*, float*, float, int, int, int);
extern "C" __global__ __launch_bounds__(640) void k_attn(const uint16_t* QKV, uint16_t* O, const uint16_t* wo, const uint16_t* w1,
                                                         const uint16_t* w2, uint16_t* wot, uint16_t* w2t, int first, int unused) {
    asm volatile("" : "+s"(QKV), "+s"(O));
    __shared__ __attribute__((aligned(16))) char lds[A_BYTES];
    const int tid = threadIdx.x, lane = tid & 63;
    const int wid = __builtin_amdgcn_readfirstlane(tid >> 6);
    const int x = blockIdx.x & 7, i = blockIdx.x >> 3;
    const int head = x * 4 + (i >> 3), qb = i & 7;
    const int r = lane & 31, h = lane >> 5;
    const unsigned lds0 = (unsigned)(uintptr_t)(lds_ptr)lds;
    const char* Qg = (const char*)QKV + head * 65536;
    const unsigned lo = lane * 16;
    if (__builtin_expect(wid >= 8, 0)) {
        const char* kv = Qg + (size_t)(wid - 7) * (SEQ * DM * 2);
        const unsigned dst = lds0 + (wid - 8) * 65536;
        asm volatile("s_barrier" ::: "memory");
#pragma unroll 4
        for (int p = 12; p < 64; ++p) glds16(lo, kv + p * 1024, dst + p * 1024);
        asm volatile("s_waitcnt vmcnt(0)\n\ts_barrier" ::: "memory");
        const int t = (i * 2 + (wid - 8)) * 64 + lane;
        const unsigned dummy = lds0 + A_Q + (wid - 8) * 1024;
        if (wid == 8) {
            if (i < 3) glds16(lo, CODE_ADDR(k_oproj) + i * 1024, dummy);
            else if (i < 10) glds16(lo, CODE_ADDR(k_ffn1) + (i - 3) * 1024, dummy);
            else if (i < 17) glds16(lo, CODE_ADDR(k_ffn2) + (i - 10) * 1024, dummy);
            else if (i < 22) glds16(lo, CODE_ADDR(k_qkv2) + (i - 17) * 1024, dummy);
        }
        if (first) {
            retile<512>(wo, wot, x * 4096 + t);
            retile<1792>(w2, w2t, x * 4096 + t);
            warm_half<512, 112>(w1, x & 1, t, dummy);
        } else {
            warm_half<1, 16384>(wot, x & 1, t, dummy);
            warm_half<512, 112>(w1, x & 1, t, dummy);
            warm_half<1, 57344>(w2t, x & 1, t, dummy);
        }
        asm volatile("s_waitcnt vmcnt(0)" ::: "memory");
        return;
    }
    {
        const char* kv = Qg + (size_t)(1 + (wid & 1)) * (SEQ * DM * 2) + (wid >> 1) * 1024;
        const unsigned dst = lds0 + (wid & 1) * 65536 + (wid >> 1) * 1024;
        glds16(lo, Qg + (qb * 8 + wid) * 1024, lds0 + A_Q + wid * 1024);
#pragma unroll
        for (int p = 0; p < 3; ++p) glds16(lo, kv + p * 4096, dst + p * 4096);
    }
    *(u32x4*)(lds + A_ONE + tid * 16) = u32x4{(tid & 7) == 0 ? 0x00003f80u : 0u, 0u, 0u, 0u};

    const bool isv = (lane & 16) == 0;
    const unsigned ka0 = lds0 + A_K + h * 512 + r * 16;
    const unsigned va0 = isv ? lds0 + A_V + (4 * h + ((lane >> 2) & 3)) * 32 + (lane & 3) * 8
                             : lds0 + A_ONE + (1 - h) * 128 + ((lane & 3) ? 8 : 0);

    wait_vm_bar<0>();
    const bf16x8 qf = lds_b128((lds_cptr)lds + A_Q + wid * 1024 + h * 512 + r * 16);
    f32x16 negm;
    {
        f32x16 z = {};
        const f32x16 s0 = MFMA32(lds_b128((lds_cptr)(uintptr_t)ka0), qf, z);
        const float m0 = -max16(s0);
#pragma unroll
        for (int e = 0; e < 16; ++e) negm[e] = m0;
    }
    f32x16 o = {};
    {
        unsigned ka = ka0, va = va0;
        const unsigned vstep = isv ? 4096u : 0u;
        int it;
        asm volatile(
            "ds_read_b128 v[136:139], %[ka]\n\t"
            "s_mov_b32 %[it], 0\n\t"
            "s_waitcnt lgkmcnt(0)\n\t"
            "v_mfma_f32_32x32x16_bf16 v[96:111], v[136:139], %[q], %[nm]\n\t"
            "ds_read_b128 v[136:139], %[ka] offset:1024\n\t"
            "ds_read_b64_tr_b16 v[140:141], %[va]\n\t"
            "ds_read_b64_tr_b16 v[142:143], %[va] offset:256\n\t"
            "ds_read_b64_tr_b16 v[144:145], %[va] offset:512\n\t"
            "ds_read_b64_tr_b16 v[146:147], %[va] offset:768\n\t"
            "s_nop 7\n\t"
            "1:\n\t"
            A_TILE(96, 112, 0) A_TILE(112, 96, 1) A_TILE(96, 112, 2) A_TILE(112, 96, 3)
            "v_add_u32 %[ka], 0x1000, %[ka]\n\t"
            "v_add_u32 %[va], %[vs], %[va]\n\t"
            "s_add_u32 %[it], %[it], 1\n\t"
            "s_cmp_lg_u32 %[it], 2\n\t"
            "s_cbranch_scc1 2f\n\t"
            "s_barrier\n\t"
            "2:\n\t"
            "s_cmp_lg_u32 %[it], 16\n\t"
            "s_cbranch_scc1 1b\n\t"
            "s_waitcnt lgkmcnt(0)\n\t"
            "s_nop 15\n\t"
            : [o] "+v"(o), [ka] "+v"(ka), [va] "+v"(va), [it] "=&s"(it)
            : [q] "v"(qf), [nm] "v"(negm), [vs] "v"(vstep)
            : "memory", "scc", "v96", "v97", "v98", "v99", "v100", "v101", "v102", "v103", "v104", "v105", "v106", "v107",
              "v108", "v109", "v110", "v111", "v112", "v113", "v114", "v115", "v116", "v117", "v118", "v119", "v120", "v121",
              "v122", "v123", "v124", "v125", "v126", "v127", "v128", "v129", "v130", "v131", "v132", "v133", "v134", "v135",
              "v136", "v137", "v138", "v139", "v140", "v141", "v142", "v143", "v144", "v145", "v146", "v147");
    }

    bool bad = false;
#pragma unroll
    for (int e = 0; e < 9; ++e) bad |= !(__builtin_fabsf(o[e]) < __builtin_inff());
    if (__any(bad)) {
        lds_cptr k2 = (lds_cptr)(uintptr_t)ka0;
        lds_cptr v2 = (lds_cptr)(uintptr_t)va0;
        const int vs2 = isv ? 1024 : 0;
        float m = -__builtin_inff();
        o = f32x16{};
#pragma unroll 1
        for (int t = 0; t < 64; ++t) {
            f32x16 z = {};
            const f32x16 s = MFMA32(lds_b128(k2), qf, z);
            const float mn = fmaxf(m, max16(s));
            const float al = __builtin_amdgcn_exp2f(m - mn);
            m = mn;
#pragma unroll
            for (int e = 0; e < 16; ++e) o[e] *= al;
            u32x4 p0, p1;
#pragma unroll
            for (int e = 0; e < 4; ++e) {
                p0[e] = pack_bf16(__builtin_amdgcn_exp2f(s[2 * e] - mn), __builtin_amdgcn_exp2f(s[2 * e + 1] - mn));
                p1[e] = pack_bf16(__builtin_amdgcn_exp2f(s[8 + 2 * e] - mn), __builtin_amdgcn_exp2f(s[8 + 2 * e + 1] - mn));
            }
            const s16x4 v00 = lds_tr(v2), v01 = lds_tr(v2 + 256), v10 = lds_tr(v2 + 512), v11 = lds_tr(v2 + 768);
            o = MFMA32(__builtin_shufflevector(v00, v01, 0, 1, 2, 3, 4, 5, 6, 7), __builtin_bit_cast(bf16x8, p0), o);
            o = MFMA32(__builtin_shufflevector(v10, v11, 0, 1, 2, 3, 4, 5, 6, 7), __builtin_bit_cast(bf16x8, p1), o);
            k2 += 1024;
            v2 += vs2;
        }
    }
    auto lsw = __builtin_amdgcn_permlane32_swap(__float_as_uint(o[8]), __float_as_uint(o[8]), false, false);
    const float rl = 1.0f / __uint_as_float(lsw[0]);
    auto sx = __builtin_amdgcn_permlane32_swap(pack_bf16(o[0] * rl, o[1] * rl), pack_bf16(o[4] * rl, o[5] * rl), false, false);
    auto sy = __builtin_amdgcn_permlane32_swap(pack_bf16(o[2] * rl, o[3] * rl), pack_bf16(o[6] * rl, o[7] * rl), false, false);
    store16_wt(O + head * 16, (unsigned)((qb * 256 + wid * 32 + r) * LDA + h * 8) * 2, u32x4{sx[0], sy[0], sx[1], sy[1]});
}

#define GEMM_ARGS const uint16_t* X, const uint16_t* W, const uint16_t* aux, uint16_t* out, float* stats, float* csg, float oscale, int xp, int rp, int op
#define GEMM_PASS X, W, aux, out, stats, csg, oscale, xp, rp, op
extern "C" __global__ __launch_bounds__(768) void k_qkv1(GEMM_ARGS) {
    gemm_body<EPI_QKV, 128, 96, 512, 1536, 1, 5, 4, true, false>(GEMM_PASS, CODE_ADDR(k_attn), 4);
}
extern "C" __global__ __launch_bounds__(768) void k_qkv2(GEMM_ARGS) {
    gemm_body<EPI_QKV, 128, 96, 512, 1536, 1, 5, 4, false, true>(GEMM_PASS, CODE_ADDR(k_attn), 4);
}
extern "C" __global__ __launch_bounds__(768) void k_oproj(GEMM_ARGS) {
    gemm_body<EPI_RES, 64, 64, 512, 512, 2, 4, 8, false, false>(GEMM_PASS, CODE_ADDR(k_ffn1), 7);
}
extern "C" __global__ __launch_bounds__(768) void k_ffn1(GEMM_ARGS) {
    gemm_body<EPI_FFN1, 128, 128, 512, 1792, 1, 4, 4, false, false>(GEMM_PASS, CODE_ADDR(k_ffn2), 7);
}
extern "C" __global__ __launch_bounds__(768) void k_ffn2(GEMM_ARGS) {
    gemm_body<EPI_RES, 64, 64, 1792, 512, 2, 4, 8, false, false>(GEMM_PASS, CODE_ADDR(k_qkv2), 5);
}

extern "C" void kernel_launch(void* const* d_in, const int* in_sizes, int n_in, void* d_out, int out_size, void* d_ws,
                              size_t ws_size, hipStream_t stream) {
    const uint16_t* x0 = (const uint16_t*)d_in[0];
    const uint16_t* wqkv = (const uint16_t*)d_in[1];
    const uint16_t* wo = (const uint16_t*)d_in[2];
    const uint16_t* w1 = (const uint16_t*)d_in[4];
    const uint16_t* b1 = (const uint16_t*)d_in[5];
    const uint16_t* w2 = (const uint16_t*)d_in[6];
    char* ws = (char*)d_ws;
    constexpr size_t MB = 1u << 20;
    uint16_t* qkv = (uint16_t*)ws;
    uint16_t* ob = (uint16_t*)(ws + 6 * MB);
    uint16_t* x1 = (uint16_t*)(ws + 9 * MB);
    uint16_t* x2 = (uint16_t*)(ws + 12 * MB);
    uint16_t* hb = (uint16_t*)(ws + 15 * MB);
    float* st1 = (float*)(ws + 23 * MB);
    float* st2 = (float*)(ws + 24 * MB);
    uint16_t* wot = (uint16_t*)(ws + 25 * MB);
    uint16_t* w2t = (uint16_t*)(ws + 26 * MB);
    float* csq = (float*)(ws + 28 * MB);
    float* cs1 = (float*)(ws + 28 * MB + 65536);
    uint16_t* out = (uint16_t*)d_out;
    constexpr float LN2 = 0.6931471805599453f;
    k_qkv1<<<256, 768, 0, stream>>>(x0, wqkv, nullptr, qkv, nullptr, csq, 1.0f, DM, 0, 0);
    k_attn<<<256, 640, 0, stream>>>(qkv, ob, wo, w1, w2, wot, w2t, 1, 0);
    k_oproj<<<256, 768, 0, stream>>>(ob, wot, x0, x1, st1, nullptr, 1.0f, LDA, DM, LDA);
    k_ffn1<<<224, 768, 0, stream>>>(x1, w1, b1, hb, st1, cs1, 1.0f, LDA, 0, 0);
    k_ffn2<<<256, 768, 0, stream>>>(hb, w2t, x1, x2, st2, nullptr, LN2, FF, LDA, LDA);
    k_qkv2<<<256, 768, 0, stream>>>(x2, wqkv, nullptr, qkv, st2, csq, 1.0f, LDA, 0, 0);
    k_attn<<<256, 640, 0, stream>>>(qkv, ob, wo, w1, w2, wot, w2t, 0, 0);
    k_oproj<<<256, 768, 0, stream>>>(ob, wot, x2, x1, st1, nullptr, 1.0f, LDA, LDA, LDA);
    k_ffn1<<<224, 768, 0, stream>>>(x1, w1, b1, hb, st1, cs1, 1.0f, LDA, 0, 0);
    k_ffn2<<<256, 768, 0, stream>>>(hb, w2t, x1, out, nullptr, nullptr, LN2, FF, LDA, DM);
}
```

```cpp
#include <hip/hip_runtime.h>
#include <hip/hip_bf16.h>
#include <stdint.h>

typedef __attribute__((ext_vector_type(8))) short bf16x8;
typedef __attribute__((ext_vector_type(4))) short s16x4;
typedef __attribute__((ext_vector_type(4))) float f32x4;
typedef __attribute__((ext_vector_type(2))) float f32x2;
typedef __attribute__((ext_vector_type(16))) float f32x16;
typedef __attribute__((ext_vector_type(4))) unsigned u32x4;
typedef __attribute__((ext_vector_type(2))) unsigned u32x2;
typedef __attribute__((ext_vector_type(2))) __bf16 bf16x2_t;
typedef __attribute__((address_space(3))) const char* lds_cptr;
typedef __attribute__((address_space(3))) char* lds_ptr;

#define DI __device__ __forceinline__
#define GAS __attribute__((address_space(1)))

constexpr int SEQ = 2048, DM = 512, NH = 32, FF = 1792;
constexpr int LDA = 576;

DI unsigned pack_bf16(float lo, float hi) {
    f32x2 v = {lo, hi};
    return __builtin_bit_cast(unsigned, __builtin_convertvector(v, bf16x2_t));
}
DI float bf_lo(unsigned u) { return __uint_as_float(u << 16); }
DI float bf_hi(unsigned u) { return __uint_as_float(u & 0xffff0000u); }
DI float dot2bf(unsigned a, unsigned b, float c) {
    return __builtin_amdgcn_fdot2_f32_bf16(__builtin_bit_cast(bf16x2_t, a), __builtin_bit_cast(bf16x2_t, b), c, false);
}
DI void glds16(unsigned voff, const void* sbase, unsigned lds_base) {
    asm volatile("s_mov_b32 m0, %2\n\ts_nop 0\n\tglobal_load_lds_dwordx4 %0, %1" ::"v"(voff), "s"(sbase), "s"(lds_base) : "memory");
}
DI void store16_wt(void* sbase, unsigned voff, u32x4 v) {
    asm volatile("global_store_dwordx4 %0, %1, %2 sc1\n\ts_nop 1" ::"v"(voff), "v"(v), "s"(sbase) : "memory");
}
DI u32x4 widen16(u32x2 a, u32x2 b) {
    auto x = __builtin_amdgcn_permlane16_swap(a[0], b[0], false, false);
    auto y = __builtin_amdgcn_permlane16_swap(a[1], b[1], false, false);
    return u32x4{x[0], y[0], x[1], y[1]};
}
DI float sum_rows(float v) {
    auto a = __builtin_amdgcn_permlane16_swap(__float_as_uint(v), __float_as_uint(v), false, false);
    v = __uint_as_float(a[0]) + __uint_as_float(a[1]);
    auto b = __builtin_amdgcn_permlane32_swap(__float_as_uint(v), __float_as_uint(v), false, false);
    return __uint_as_float(b[0]) + __uint_as_float(b[1]);
}
DI s16x4 lds_tr(lds_cptr p) {
    typedef short v4i16_t __attribute__((ext_vector_type(4)));
    return __builtin_bit_cast(s16x4, __builtin_amdgcn_ds_read_tr16_b64_v4i16((__attribute__((address_space(3))) v4i16_t*)p));
}
DI bf16x8 lds_b128(lds_cptr p) { return *(const __attribute__((address_space(3))) bf16x8*)p; }
template <int N> DI void wait_vm_bar() {
    __builtin_amdgcn_s_waitcnt(0xC07F);
    asm volatile("s_waitcnt vmcnt(%0) lgkmcnt(0)\n\ts_barrier" ::"i"(N) : "memory");
}

DI void bar_lds() {
    __builtin_amdgcn_s_waitcnt(0xC07F);
    asm volatile("s_waitcnt lgkmcnt(0)\n\ts_barrier" ::: "memory");
}

#define CODE_ADDR(sym) ({ unsigned long long a_;                                                                                  \
    asm volatile("s_getpc_b64 s[98:99]\n\ts_add_u32 s98, s98, " #sym "@rel32@lo+4\n\ts_addc_u32 s99, s99, " #sym "@rel32@hi+12\n\t" \
                 "s_mov_b64 %0, s[98:99]" : "=s"(a_) :: "scc", "s98", "s99"); (const char*)a_; })

#define MFMA16(a, b, c) __builtin_amdgcn_mfma_f32_16x16x32_bf16((a), (b), (c), 0, 0, 0)
#define MFMA32(a, b, c) __builtin_amdgcn_mfma_f32_32x32x16_bf16((a), (b), (c), 0, 0, 0)

enum { EPI_QKV = 0, EPI_FFN1 = 1, EPI_RES = 2 };
#define LD_INIT 2

template <int PB> DI int wswz(int row) {
    if (PB == 64) return (row >> 3) & 1;
    if (PB == 128) return ((row >> 1) & 1) | (((row >> 3) & 1) << 1);
    return (row & 3) | (((row >> 3) & 1) << 2);
}

template <int EPI, int BM, int BN, int KT, int N, int WK, int NSLOT, int TMX  , bool SELF, bool CSIN>
DI void gemm_body(const uint16_t* X, const uint16_t* W, const uint16_t* aux  , uint16_t* out,
                  float* stats, float* csg, float oscale, int xp, int rp, int op  ,
                  const char* next_code, int next_kb) {
    asm volatile("" : "+s"(X), "+s"(W), "+s"(aux), "+s"(out), "+s"(stats), "+s"(csg), "+s"(oscale), "+s"(xp), "+s"(rp), "+s"(op));
    constexpr bool LN = (EPI != EPI_RES);
    constexpr int WP = LN ? N : BN;
    constexpr int TN = (WK == 1) ? BN / 32 : 2;
    constexpr int PB = (BN == 96) ? 64 : BN * 2;
    constexpr int SPR = PB / 32;
    constexpr int XB = BM * 128, WB = 64 * BN * 2;
    constexpr int SLOT = WK * (XB + WB);
    constexpr int NC = KT / (64 * WK);
    constexpr int PW = WK * WB / 4096;
    constexpr int NTN = N / BN, NTM = SEQ / BM;
    constexpr int RING = NSLOT * SLOT;
    constexpr int LDS_BYTES = RING + 3072;
    static_assert(WK * XB == 16384, "x pieces per slot must be 16");
    __shared__ __attribute__((aligned(16))) char lds[LDS_BYTES];

    const int tid = threadIdx.x, lane = tid & 63;
    const int wid = __builtin_amdgcn_readfirstlane(tid >> 6);
    int tm, tn;
    {
        const int x = blockIdx.x & 7, i = blockIdx.x >> 3;
        constexpr int XM = NTM / TMX;
        constexpr int XN = 8 / XM;
        constexpr int TNX = NTN / XN;
        tm = (x / XN) * TMX + (i % TMX);
        tn = (x % XN) * TNX + (i / TMX);
    }
    const int row0 = tm * BM, n0 = tn * BN;
    const unsigned lds0 = (unsigned)(uintptr_t)(lds_ptr)lds;

    if (__builtin_expect(wid < 4, 1)) {
        constexpr int PER = 4 + PW;
        const int l = wid;
        const int xr = 8 * l + (lane >> 3);
        const unsigned xoff = (unsigned)(xr * xp * 2 + (((lane & 7) ^ ((xr >> 1) & 7)) << 4));
        constexpr int RPP = 1024 / PB;
        const int wr = RPP * l + lane / (PB / 16), wc = lane % (PB / 16);
        const unsigned woff = (unsigned)(wr * WP * 2 + (((wc >> 1) ^ wswz<PB>(wr)) << 5) + ((wc & 1) << 4));
        constexpr int WSTEP = (BN == 96) ? 64 : 4 * RPP * WP * 2;
        const char* xg = (const char*)(X + (size_t)row0 * xp);
        const char* wg = (const char*)(LN ? W + n0 : W + (size_t)tn * (KT * BN));
        const int xstep = 64 * xp;
        auto issue = [&](int c) {
            const unsigned sb = lds0 + (c % NSLOT) * SLOT + l * 1024;
            const int kc = SELF ? ((c + tm) & (NC - 1)) : c;
            const char* xs = xg + (size_t)kc * (WK * 128);
            const char* ws = wg + (size_t)kc * (WK * 64 * WP * 2);
#pragma unroll
            for (int i = 0; i < 4; ++i)
                glds16(xoff, xs + ((WK == 1) ? i * xstep : (i & 1) * xstep + (i >> 1) * 128), sb + i * 4096);
#pragma unroll
            for (int i = 0; i < PW; ++i) glds16(woff, ws + i * WSTEP, sb + 16384 + i * 4096);
        };
#pragma unroll
        for (int c = 0; c < LD_INIT; ++c) issue(c);
#pragma unroll
        for (int b = -1; b < NC - 1; ++b) {
            constexpr auto nis = [](int bb) { int n = LD_INIT; for (int t = -1; t < bb; ++t) { int cap = t + NSLOT + 1; n = (n + 1 < cap) ? n + 1 : cap; if (n > NC) n = NC; } return n; };
            const int before = nis(b), after = nis(b + 1);
            const int rem = before - (b + 2);
            if (rem == 0) wait_vm_bar<0>(); else if (rem == 1) wait_vm_bar<PER>(); else if (rem == 2) wait_vm_bar<2 * PER>();
            else if (rem == 3) wait_vm_bar<3 * PER>(); else wait_vm_bar<4 * PER>();
#pragma unroll
            for (int c = 0; c < NC; ++c) if (c >= before && c < after) issue(c);
        }
        if (l == 0 && (int)(blockIdx.x >> 3) < next_kb)
            glds16(lane * 16, next_code + (blockIdx.x >> 3) * 1024, lds0 + RING + 2048);
        asm volatile("s_waitcnt vmcnt(0)" ::: "memory");
        return;
    }

    const int cw = wid - 4;
    const int r = lane & 15, g = lane >> 4;
    const int wk = (WK == 1) ? 0 : (cw >> 2);
    const int wm = (WK == 1) ? (cw >> 1) : ((cw >> 1) & 1);
    const int wn = cw & 1;
    const int mrot = LN ? wn : wk;
    const int jrot = LN ? (wm % TN) : 0;
    int jb[TN];
#pragma unroll
    for (int j = 0; j < TN; ++j) jb[j] = (j + jrot) % TN;
    const int mytok = row0 + wm * 32 + mrot * 16 + r;

    f32x4 pst[2];
    u32x2 auxv[TN];
    if (LN && !SELF) {
        const GAS f32x4* sp = (const GAS f32x4*)(stats + (size_t)mytok * 32 + g * 8);
        pst[0] = sp[0]; pst[1] = sp[1];
    }
    f32x4 csv[TN];
    if (LN && CSIN) {
#pragma unroll
        for (int j = 0; j < TN; ++j) csv[j] = *(const GAS f32x4*)(csg + n0 + wn * (BN / 2) + jb[j] * 16 + g * 4);
    }
    if (EPI == EPI_FFN1) {
#pragma unroll
        for (int j = 0; j < TN; ++j) auxv[j] = *(const GAS u32x2*)(aux + n0 + wn * (BN / 2) + jb[j] * 16 + g * 4);
    }
    if (EPI == EPI_RES) {
#pragma unroll
        for (int j = 0; j < 2; ++j) auxv[j] = *(const GAS u32x2*)(aux + (size_t)mytok * rp + n0 + wn * 32 + j * 16 + g * 4);
    }

    const int xrow = wm * 32 + r;
    const int xsw = (xrow >> 1) & 7;
    constexpr int NG = (NSLOT + 1) / 2;
    unsigned xa[NG][2][2], wa[NG][TN];
    {
        const int q = (lane >> 2) & 3, p = lane & 3;
        const int wrow = 8 * g + q;
#pragma unroll
        for (int gi = 0; gi < NG; ++gi) {
#pragma unroll
            for (int s = 0; s < 2; ++s)
#pragma unroll
                for (int m = 0; m < 2; ++m) {
                    xa[gi][s][m] = lds0 + gi * 2 * SLOT + wk * XB + (xrow + ((m ^ mrot) << 4)) * 128 + (((4 * s + g) ^ xsw) << 4);
                    asm volatile("" : "+v"(xa[gi][s][m]));
                }
#pragma unroll
            for (int j = 0; j < TN; ++j) {
                const int c16 = wn * TN + jb[j];
                wa[gi][j] = lds0 + gi * 2 * SLOT + WK * XB + wk * WB + (c16 / SPR) * (64 * PB) + wrow * PB +
                            (((c16 % SPR) ^ wswz<PB>(wrow)) << 5) + p * 8;
                asm volatile("" : "+v"(wa[gi][j]));
            }
        }
    }

    float* st = (float*)(lds + RING);
    float* cs = (float*)(lds + RING + 1024);
    auto put_stats = [&](float s1, float sq) {
        const float mu = s1 * (1.0f / KT);
        const float var = fmaxf(sq * (1.0f / KT) - mu * mu, 0.f);
        const float rs = __builtin_amdgcn_rsqf(var + 1e-5f);
        if (g == (r >> 2)) *(f32x2*)(st + (wm * 32 + mrot * 16 + r) * 2) = f32x2{mu * rs, rs};
    };

    f32x4 acc[2][TN];
#pragma unroll
    for (int m = 0; m < 2; ++m)
#pragma unroll
        for (int j = 0; j < TN; ++j) acc[m][j] = f32x4{0.f, 0.f, 0.f, 0.f};
    f32x4 xs1 = {0.f, 0.f, 0.f, 0.f}, xs2 = {0.f, 0.f, 0.f, 0.f}, wcs = {0.f, 0.f, 0.f, 0.f};
    const bf16x8 ones = {0x3f80, 0x3f80, 0x3f80, 0x3f80, 0x3f80, 0x3f80, 0x3f80, 0x3f80};

    bf16x8 xf[2][2][2], wf[2][2][TN];
    auto readf = [&](int c) {
        const int gi = (c % NSLOT) / 2, so = ((c % NSLOT) & 1) * SLOT, b = c & 1;
#pragma unroll
        for (int s = 0; s < 2; ++s) {
#pragma unroll
            for (int m = 0; m < 2; ++m) xf[b][s][m] = lds_b128((lds_cptr)(uintptr_t)xa[gi][s][m] + so);
#pragma unroll
            for (int j = 0; j < TN; ++j) {
                const s16x4 lo = lds_tr((lds_cptr)(uintptr_t)wa[gi][j] + so + (32 * s) * PB);
                const s16x4 hi = lds_tr((lds_cptr)(uintptr_t)wa[gi][j] + so + (32 * s + 4) * PB);
                wf[b][s][j] = __builtin_shufflevector(lo, hi, 0, 1, 2, 3, 4, 5, 6, 7);
            }
        }
    };
    auto fold_mfma = [&](int b) {
#pragma unroll
        for (int s = 0; s < 2; ++s) {
            if (LN && SELF) {
                xs1 = MFMA16(ones, xf[b][s][0], xs1);
                xs2 = MFMA16(xf[b][s][0], xf[b][s][0], xs2);
            }
            if (LN && !CSIN) wcs = MFMA16(wf[b][s][0], ones, wcs);
        }
    };
    auto main_mfma = [&](int b) {
#pragma unroll
        for (int s = 0; s < 2; ++s)
#pragma unroll
            for (int m = 0; m < 2; ++m)
#pragma unroll
                for (int j = 0; j < TN; ++j) acc[m][j] = MFMA16(wf[b][s][j], xf[b][s][m], acc[m][j]);
    };
    bar_lds();
    readf(0);
#pragma unroll
    for (int c = 0; c < NC - 1; ++c) {
        if (c == NC - 2) {
            main_mfma(c & 1);
            fold_mfma(c & 1);
            __builtin_amdgcn_sched_barrier(0);
            bar_lds();
            readf(c + 1);
            break;
        }
        bar_lds();
        __builtin_amdgcn_sched_barrier(0);
        readf(c + 1);
        main_mfma(c & 1);
        fold_mfma(c & 1);
        constexpr int NM = 2 * (2 * TN + ((LN && SELF) ? 2 : 0) + ((LN && !CSIN) ? 1 : 0)), NR = 2 * (2 + 2 * TN);
        constexpr int RPM = (WK == 1) ? 2 : 3;
#pragma unroll
        for (int i = 0; i < NM; ++i) {
            __builtin_amdgcn_sched_group_barrier(0x008, 1, 0);
            if (i * RPM < NR) __builtin_amdgcn_sched_group_barrier(0x100, RPM, 0);
        }
        __builtin_amdgcn_sched_barrier(0);
        if (LN && !SELF && c == NC - 3) {
            float s1 = (pst[0][0] + pst[0][2]) + (pst[1][0] + pst[1][2]);
            float sq = (pst[0][1] + pst[0][3]) + (pst[1][1] + pst[1][3]);
            put_stats(sum_rows(s1), sum_rows(sq));
            __builtin_amdgcn_sched_barrier(0);
        }
    }
    fold_mfma((NC - 1) & 1);
    __builtin_amdgcn_sched_barrier(0);
    main_mfma((NC - 1) & 1);
    if (LN) {
        if (SELF) put_stats(xs1[0], (r & 2) ? ((r & 1) ? xs2[3] : xs2[2]) : ((r & 1) ? xs2[1] : xs2[0]));
        if (!CSIN && r == 0 && wm < TN) {
            *(f32x4*)(cs + wn * (BN / 2) + jrot * 16 + g * 4) = wcs;
            if (tm == 0) *(GAS f32x4*)(csg + n0 + wn * (BN / 2) + jrot * 16 + g * 4) = wcs;
        }
    }

    if (LN) {
        if (SELF || !CSIN) bar_lds();
        const unsigned stok = row0 + wm * 32 + (((g & 1) ^ mrot) << 4) + r;
        f32x2 ms[2];
#pragma unroll
        for (int m = 0; m < 2; ++m) ms[m] = *(const f32x2*)(st + (wm * 32 + ((m ^ mrot) << 4) + r) * 2);
#pragma unroll
        for (int j = 0; j < TN; ++j) {
            const int nl = wn * (BN / 2) + jb[j] * 16 + g * 4;
            const f32x4 c4 = CSIN ? csv[j] : *(const f32x4*)(cs + nl);
            if (EPI == EPI_QKV) {
                const int blk = (n0 + wn * (BN / 2)) / 16 + jb[j];
                const int sec = blk >> 5, head = blk & 31;
                const float sc = sec == 0 ? 0.25f * 1.4426950408889634f : 1.0f;
                char* base = (char*)out + (size_t)sec * (SEQ * DM * 2) + head * 65536;
                u32x2 o[2];
#pragma unroll
                for (int m = 0; m < 2; ++m) {
                    const float a = ms[m][1] * sc, bq = ms[m][0] * sc;
                    float v[4];
#pragma unroll
                    for (int e = 0; e < 4; ++e) v[e] = acc[m][j][e] * a - bq * c4[e];
                    o[m] = u32x2{pack_bf16(v[0], v[1]), pack_bf16(v[2], v[3])};
                }
                const unsigned off = sec < 2 ? (stok >> 5) * 1024 + (g >> 1) * 512 + (stok & 31) * 16 : stok * 32 + (g >> 1) * 16;
                store16_wt(base, off, widen16(o[0], o[1]));
            } else {
                constexpr float L2E = 1.4426950408889634f;
                const float bb[4] = {bf_lo(auxv[j][0]) * L2E, bf_hi(auxv[j][0]) * L2E, bf_lo(auxv[j][1]) * L2E, bf_hi(auxv[j][1]) * L2E};
                u32x2 o[2];
#pragma unroll
                for (int m = 0; m < 2; ++m) {
                    const float a = ms[m][1] * L2E, bq = ms[m][0] * L2E;
#pragma unroll
                    for (int e = 0; e < 4; e += 2) {
                        const f32x2 u = f32x2{acc[m][j][e], acc[m][j][e + 1]} * a + (f32x2{bb[e], bb[e + 1]} - f32x2{c4[e], c4[e + 1]} * bq);
                        const f32x2 ex = {__builtin_amdgcn_exp2f(u[0]), __builtin_amdgcn_exp2f(u[1])};
                        const f32x2 d = ex * (ex + 2.f) + 2.f;
                        const f32x2 rc = {__builtin_amdgcn_rcpf(d[0]), __builtin_amdgcn_rcpf(d[1])};
                        const f32x2 v = (u * rc) * -2.f + u;
                        o[m][e >> 1] = pack_bf16(v[0], v[1]);
                    }
                }
#pragma unroll
                for (int m = 0; m < 2; ++m)
                    *(u32x2*)(lds + cw * 4608 + ((((m ^ mrot) << 4) + r) * 144 + jb[j] * 32 + g * 8)) = o[m];
            }
        }
        if (EPI == EPI_FFN1) {
#pragma unroll
            for (int i = 0; i < 4; ++i) {
                const int row = i * 8 + (lane >> 3);
                const u32x4 v = *(const u32x4*)(lds + cw * 4608 + row * 144 + (lane & 7) * 16);
                store16_wt(out + n0 + wn * (BN / 2), (unsigned)((row0 + wm * 32 + row) * N + (lane & 7) * 8) * 2, v);
            }
        }
    } else {
        f32x4* ex = (f32x4*)(lds + ((NC - 2) % NSLOT) * SLOT);
#pragma unroll
        for (int j = 0; j < 2; ++j) ex[(cw * 2 + j) * 64 + lane] = acc[1][j];
        bar_lds();
        float s1 = 0.f, s2 = 0.f;
        u32x2 ov[2];
#pragma unroll
        for (int j = 0; j < 2; ++j) {
            const f32x4 o4 = ex[((cw ^ 4) * 2 + j) * 64 + lane];
            const f32x4 mine = acc[0][j];
            const float v0 = fmaf(mine[0] + o4[0], oscale, bf_lo(auxv[j][0])), v1 = fmaf(mine[1] + o4[1], oscale, bf_hi(auxv[j][0]));
            const float v2 = fmaf(mine[2] + o4[2], oscale, bf_lo(auxv[j][1])), v3 = fmaf(mine[3] + o4[3], oscale, bf_hi(auxv[j][1]));
            const u32x2 o = {pack_bf16(v0, v1), pack_bf16(v2, v3)};
            ov[j] = o;
            const float q0 = bf_lo(o[0]), q1 = bf_hi(o[0]), q2 = bf_lo(o[1]), q3 = bf_hi(o[1]);
            s1 += (q0 + q1) + (q2 + q3);
            s2 = fmaf(q0, q0, fmaf(q1, q1, fmaf(q2, q2, fmaf(q3, q3, s2))));
        }
        store16_wt(out + n0 + wn * 32, (unsigned)(mytok * op + (g & 1) * 16 + (g >> 1) * 8) * 2, widen16(ov[0], ov[1]));
        if (stats) {
            s1 = sum_rows(s1); s2 = sum_rows(s2);
            if (g == 0) *(GAS f32x2*)(stats + (size_t)mytok * 32 + (tn * 2 + wn) * 2) = f32x2{s1, s2};
        }
    }
}

constexpr int A_K = 0, A_V = 65536, A_ONE = 131072, A_Q = A_ONE + 8192, A_BYTES = A_Q + 8192;

#define PRIO_MASK 0x8888u
DI float max16(const f32x16& s) {
    float a = fmaxf(fmaxf(s[0], s[1]), s[2]), b = fmaxf(fmaxf(s[3], s[4]), s[5]);
    a = fmaxf(fmaxf(a, s[6]), s[7]); b = fmaxf(fmaxf(b, s[8]), s[9]);
    a = fmaxf(fmaxf(a, s[10]), s[11]); b = fmaxf(fmaxf(b, s[12]), s[13]);
    a = fmaxf(fmaxf(a, s[14]), s[15]);
    a = fmaxf(a, b);
    auto rr = __builtin_amdgcn_permlane32_swap(__float_as_uint(a), __float_as_uint(a), false, false);
    return fmaxf(__uint_as_float(rr[0]), __uint_as_float(rr[1]));
}

#define A_EXP(S, i) "v_exp_f32 v[" #S "+" #i "], v[" #S "+" #i "]\n\t"
#define A_CVT(P, S, i) "v_cvt_pk_bf16_f32 v[" #P "], v[" #S "+" #i "], v[" #S "+" #i "+1]\n\t"
#define A_TILE(SC, SN, U)                                                                              \
    "s_waitcnt lgkmcnt(4)\n\t"                                                                         \
    "v_mfma_f32_32x32x16_bf16 v[" #SN ":" #SN "+15], v[136:139], %[q], %[nm]\n\t"                      \
    "ds_read_b128 v[136:139], %[ka] offset:(" #U "+2)*1024\n\t"                                        \
    A_EXP(SC, 0) A_EXP(SC, 1) A_EXP(SC, 2) A_EXP(SC, 3) A_EXP(SC, 4) A_EXP(SC, 5) A_EXP(SC, 6) A_EXP(SC, 7) \
    A_CVT(128, SC, 0) A_CVT(129, SC, 2) A_CVT(130, SC, 4) A_CVT(131, SC, 6)                            \
    A_EXP(SC, 8) A_EXP(SC, 9)                                                                          \
    "s_waitcnt lgkmcnt(3)\n\t"                                                                         \
    "v_mfma_f32_32x32x16_bf16 %[o], v[140:143], v[128:131], %[o]\n\t"                                  \
    "ds_read_b64_tr_b16 v[140:141], %[va] offset:(" #U "+1)*1024\n\t"                                  \
    "ds_read_b64_tr_b16 v[142:143], %[va] offset:(" #U "+1)*1024+256\n\t"                              \
    A_EXP(SC, 10) A_EXP(SC, 11) A_EXP(SC, 12) A_EXP(SC, 13) A_EXP(SC, 14) A_EXP(SC, 15)                \
    A_CVT(132, SC, 8) A_CVT(133, SC, 10) A_CVT(134, SC, 12) A_CVT(135, SC, 14)                         \
    "s_waitcnt lgkmcnt(3)\n\t"                                                                         \
    "s_nop 0\n\t"                                                                                      \
    "v_mfma_f32_32x32x16_bf16 %[o], v[144:147], v[132:135], %[o]\n\t"                                  \
    "ds_read_b64_tr_b16 v[144:145], %[va] offset:(" #U "+1)*1024+512\n\t"                              \
    "ds_read_b64_tr_b16 v[146:147], %[va] offset:(" #U "+1)*1024+768\n\t"

DI void retile(const uint16_t* wo, const uint16_t* w2, uint16_t* wot, uint16_t* w2t, int gw, int lane) {
    const bool isw = gw < 512, third = gw < 256;
    const char* s0 = isw ? (const char*)wo + gw * 1024 : (const char*)w2 + (gw - 512) * 1024;
    char* d0 = isw ? (char*)wot + gw * 128 : (char*)w2t + (gw - 512) * 128;
    u32x4 v0 = *(const GAS u32x4*)(s0 + lane * 16);
    u32x4 v1 = *(const GAS u32x4*)((const char*)w2 + (gw + 512) * 1024 + lane * 16), v2;
    if (third) v2 = *(const GAS u32x4*)((const char*)w2 + (gw + 1536) * 1024 + lane * 16);
    const unsigned o2 = (unsigned)(lane >> 3) * (1792 * 128) + (lane & 7) * 16;
    store16_wt(d0, isw ? (unsigned)(lane >> 3) * (512 * 128) + (lane & 7) * 16 : o2, v0);
    store16_wt((char*)w2t + (gw + 512) * 128, o2, v1);
    if (third) store16_wt((char*)w2t + (gw + 1536) * 128, o2, v2);
}

extern "C" __global__ __launch_bounds__(768) void k_attn(const uint16_t* QKV, uint16_t* O, const uint16_t* wo, const uint16_t* w1,
                                                         const uint16_t* w2, uint16_t* wot, uint16_t* w2t, int first, int unused) {
    asm volatile("" : "+s"(QKV), "+s"(O));
    __shared__ __attribute__((aligned(16))) char lds[A_BYTES];
    const int tid = threadIdx.x, lane = tid & 63;
    const int wid = __builtin_amdgcn_readfirstlane(tid >> 6);
    const int x = blockIdx.x & 7, i = blockIdx.x >> 3;
    const int head = x * 4 + (i >> 3), qb = i & 7;
    const int r = lane & 31, h = lane >> 5;
    const unsigned lds0 = (unsigned)(uintptr_t)(lds_ptr)lds;
    const char* Qg = (const char*)QKV + head * 65536;
    const unsigned lo = lane * 16;
    if (__builtin_expect(wid >= 8, 0)) {
        const int l = wid - 8;
        const char* kv = Qg + (size_t)(1 + (l >> 1)) * (SEQ * DM * 2) + (l & 1) * 1024;
        const unsigned dst = lds0 + (l >> 1) * 65536 + (l & 1) * 1024;
        __builtin_amdgcn_s_setprio(3);
        asm volatile("s_barrier" ::: "memory");
#pragma unroll 2
        for (int p = 6; p < 32; ++p) glds16(lo, kv + p * 2048, dst + p * 2048);
        asm volatile("s_waitcnt vmcnt(0)\n\ts_barrier" ::: "memory");
        const int xw = i * 4 + l;
        const unsigned dummy = lds0 + A_Q + l * 1024;
        if (l == 0) {
            if (i < 3) glds16(lo, CODE_ADDR(k_oproj) + i * 1024, dummy);
            else if (i < 10) glds16(lo, CODE_ADDR(k_ffn1) + (i - 3) * 1024, dummy);
            else if (i < 17) glds16(lo, CODE_ADDR(k_ffn2) + (i - 10) * 1024, dummy);
            else if (i < 22) glds16(lo, CODE_ADDR(k_qkv2) + (i - 17) * 1024, dummy);
        }
        if (first) {
            retile(wo, w2, wot, w2t, x * 128 + xw, lane);
        } else {
            const char* a = (const char*)wot + (x & 1) * 262144 + xw * 2048;
            glds16(lo, a, dummy); glds16(lo, a + 1024, dummy);
            const char* c = (const char*)w2t + (x & 1) * 917504 + xw * 7168;
#pragma unroll
            for (int k = 0; k < 7; ++k) glds16(lo, c + k * 1024, dummy);
        }
        {
            const char* bw = (const char*)w1 + (x & 1) * 1792 + xw * (4 * 3584);
            const unsigned l56 = (lane < 56 ? lane : 55) * 16;
#pragma unroll
            for (int k = 0; k < 8; ++k) glds16(l56, bw + (k >> 1) * 3584 + (k & 1) * 896, dummy);
        }
        asm volatile("s_waitcnt vmcnt(0)" ::: "memory");
        return;
    }
    {
        const char* kv = Qg + (size_t)(1 + (wid & 1)) * (SEQ * DM * 2) + (wid >> 1) * 1024;
        const unsigned dst = lds0 + (wid & 1) * 65536 + (wid >> 1) * 1024;
        glds16(lo, Qg + (qb * 8 + wid) * 1024, lds0 + A_Q + wid * 1024);
#pragma unroll
        for (int p = 0; p < 3; ++p) glds16(lo, kv + p * 4096, dst + p * 4096);
    }
    *(u32x4*)(lds + A_ONE + tid * 16) = u32x4{(tid & 7) == 0 ? 0x00003f80u : 0u, 0u, 0u, 0u};

    const bool isv = (lane & 16) == 0;
    const unsigned ka0 = lds0 + A_K + h * 512 + r * 16;
    const unsigned va0 = isv ? lds0 + A_V + (4 * h + ((lane >> 2) & 3)) * 32 + (lane & 3) * 8
                             : lds0 + A_ONE + (1 - h) * 128 + ((lane & 3) ? 8 : 0);

    wait_vm_bar<0>();
    const bf16x8 qf = lds_b128((lds_cptr)lds + A_Q + wid * 1024 + h * 512 + r * 16);
    f32x16 negm;
    {
        f32x16 z = {};
        const f32x16 s0 = MFMA32(lds_b128((lds_cptr)(uintptr_t)ka0), qf, z);
        const float m0 = -max16(s0);
#pragma unroll
        for (int e = 0; e < 16; ++e) negm[e] = m0;
    }
    f32x16 o = {};
    {
        unsigned ka = ka0, va = va0;
        const unsigned vstep = isv ? 4096u : 0u;
        const unsigned pmask = wid < 4 ? PRIO_MASK : 0u;
        if (wid < 4) __builtin_amdgcn_s_setprio(2); else __builtin_amdgcn_s_setprio(1);
        int it;
        asm volatile(
            "ds_read_b128 v[136:139], %[ka]\n\t"
            "s_mov_b32 %[it], 0\n\t"
            "s_waitcnt lgkmcnt(0)\n\t"
            "v_mfma_f32_32x32x16_bf16 v[96:111], v[136:139], %[q], %[nm]\n\t"
            "ds_read_b128 v[136:139], %[ka] offset:1024\n\t"
            "ds_read_b64_tr_b16 v[140:141], %[va]\n\t"
            "ds_read_b64_tr_b16 v[142:143], %[va] offset:256\n\t"
            "ds_read_b64_tr_b16 v[144:145], %[va] offset:512\n\t"
            "ds_read_b64_tr_b16 v[146:147], %[va] offset:768\n\t"
            "s_nop 7\n\t"
            "1:\n\t"
            A_TILE(96, 112, 0) A_TILE(112, 96, 1) A_TILE(96, 112, 2) A_TILE(112, 96, 3)
            "v_add_u32 %[ka], 0x1000, %[ka]\n\t"
            "v_add_u32 %[va], %[vs], %[va]\n\t"
            "s_add_u32 %[it], %[it], 1\n\t"
            "s_cmp_lg_u32 %[it], 2\n\t"
            "s_cbranch_scc1 2f\n\t"
            "s_barrier\n\t"
            "2:\n\t"
            "s_cmp_eq_u32 %[pm], 0\n\t"
            "s_cbranch_scc1 3f\n\t"
            "s_bitcmp1_b32 %[pm], %[it]\n\t"
            "s_cbranch_scc1 4f\n\t"
            "s_setprio 2\n\t"
            "s_branch 3f\n\t"
            "4:\n\t"
            "s_setprio 0\n\t"
            "3:\n\t"
            "s_cmp_lg_u32 %[it], 16\n\t"
            "s_cbranch_scc1 1b\n\t"
            "s_waitcnt lgkmcnt(0)\n\t"
            "s_nop 15\n\t"
            : [o] "+v"(o), [ka] "+v"(ka), [va] "+v"(va), [it] "=&s"(it)
            : [q] "v"(qf), [nm] "v"(negm), [vs] "v"(vstep), [pm] "s"(pmask)
            : "memory", "scc", "v96", "v97", "v98", "v99", "v100", "v101", "v102", "v103", "v104", "v105", "v106", "v107",
              "v108", "v109", "v110", "v111", "v112", "v113", "v114", "v115", "v116", "v117", "v118", "v119", "v120", "v121",
              "v122", "v123", "v124", "v125", "v126", "v127", "v128", "v129", "v130", "v131", "v132", "v133", "v134", "v135",
              "v136", "v137", "v138", "v139", "v140", "v141", "v142", "v143", "v144", "v145", "v146", "v147");
    }

    bool bad = false;
#pragma unroll
    for (int e = 0; e < 9; ++e) bad |= !(__builtin_fabsf(o[e]) < __builtin_inff());
    if (__any(bad)) {
        lds_cptr k2 = (lds_cptr)(uintptr_t)ka0;
        lds_cptr v2 = (lds_cptr)(uintptr_t)va0;
        const int vs2 = isv ? 1024 : 0;
        float m = -__builtin_inff();
        o = f32x16{};
#pragma unroll 1
        for (int t = 0; t < 64; ++t) {
            f32x16 z = {};
            const f32x16 s = MFMA32(lds_b128(k2), qf, z);
            const float mn = fmaxf(m, max16(s));
            const float al = __builtin_amdgcn_exp2f(m - mn);
            m = mn;
#pragma unroll
            for (int e = 0; e < 16; ++e) o[e] *= al;
            u32x4 p0, p1;
#pragma unroll
            for (int e = 0; e < 4; ++e) {
                p0[e] = pack_bf16(__builtin_amdgcn_exp2f(s[2 * e] - mn), __builtin_amdgcn_exp2f(s[2 * e + 1] - mn));
                p1[e] = pack_bf16(__builtin_amdgcn_exp2f(s[8 + 2 * e] - mn), __builtin_amdgcn_exp2f(s[8 + 2 * e + 1] - mn));
            }
            const s16x4 v00 = lds_tr(v2), v01 = lds_tr(v2 + 256), v10 = lds_tr(v2 + 512), v11 = lds_tr(v2 + 768);
            o = MFMA32(__builtin_shufflevector(v00, v01, 0, 1, 2, 3, 4, 5, 6, 7), __builtin_bit_cast(bf16x8, p0), o);
            o = MFMA32(__builtin_shufflevector(v10, v11, 0, 1, 2, 3, 4, 5, 6, 7), __builtin_bit_cast(bf16x8, p1), o);
            k2 += 1024;
            v2 += vs2;
        }
    }
    auto lsw = __builtin_amdgcn_permlane32_swap(__float_as_uint(o[8]), __float_as_uint(o[8]), false, false);
    const float rl = 1.0f / __uint_as_float(lsw[0]);
    auto sx = __builtin_amdgcn_permlane32_swap(pack_bf16(o[0] * rl, o[1] * rl), pack_bf16(o[4] * rl, o[5] * rl), false, false);
    auto sy = __builtin_amdgcn_permlane32_swap(pack_bf16(o[2] * rl, o[3] * rl), pack_bf16(o[6] * rl, o[7] * rl), false, false);
    store16_wt(O + head * 16, (unsigned)((qb * 256 + wid * 32 + r) * LDA + h * 8) * 2, u32x4{sx[0], sy[0], sx[1], sy[1]});
}

#define GEMM_ARGS const uint16_t* X, const uint16_t* W, const uint16_t* aux, uint16_t* out, float* stats, float* csg, float oscale, int xp, int rp, int op
#define GEMM_PASS X, W, aux, out, stats, csg, oscale, xp, rp, op
extern "C" __global__ __launch_bounds__(768) void k_qkv1(GEMM_ARGS) {
    gemm_body<EPI_QKV, 128, 96, 512, 1536, 1, 5, 4, true, false>(GEMM_PASS, CODE_ADDR(k_attn), 4);
}
extern "C" __global__ __launch_bounds__(768) void k_qkv2(GEMM_ARGS) {
    gemm_body<EPI_QKV, 128, 96, 512, 1536, 1, 5, 4, false, true>(GEMM_PASS, CODE_ADDR(k_attn), 4);
}
extern "C" __global__ __launch_bounds__(768) void k_oproj(GEMM_ARGS) {
    gemm_body<EPI_RES, 64, 64, 512, 512, 2, 4, 8, false, false>(GEMM_PASS, CODE_ADDR(k_ffn1), 7);
}
extern "C" __global__ __launch_bounds__(768) void k_ffn1(GEMM_ARGS) {
    gemm_body<EPI_FFN1, 128, 128, 512, 1792, 1, 4, 4, false, false>(GEMM_PASS, CODE_ADDR(k_ffn2), 7);
}
extern "C" __global__ __launch_bounds__(768) void k_ffn2(GEMM_ARGS) {
    gemm_body<EPI_RES, 64, 64, 1792, 512, 2, 4, 8, false, false>(GEMM_PASS, CODE_ADDR(k_qkv2), 5);
}

extern "C" void kernel_launch(void* const* d_in, const int* in_sizes, int n_in, void* d_out, int out_size, void* d_ws,
                              size_t ws_size, hipStream_t stream) {
    const uint16_t* x0 = (const uint16_t*)d_in[0];
    const uint16_t* wqkv = (const uint16_t*)d_in[1];
    const uint16_t* wo = (const uint16_t*)d_in[2];
    const uint16_t* w1 = (const uint16_t*)d_in[4];
    const uint16_t* b1 = (const uint16_t*)d_in[5];
    const uint16_t* w2 = (const uint16_t*)d_in[6];
    char* ws = (char*)d_ws;
    constexpr size_t MB = 1u << 20;
    uint16_t* qkv = (uint16_t*)ws;
    uint16_t* ob = (uint16_t*)(ws + 6 * MB);
    uint16_t* x1 = (uint16_t*)(ws + 9 * MB);
    uint16_t* x2 = (uint16_t*)(ws + 12 * MB);
    uint16_t* hb = (uint16_t*)(ws + 15 * MB);
    float* st1 = (float*)(ws + 23 * MB);
    float* st2 = (float*)(ws + 24 * MB);
    uint16_t* wot = (uint16_t*)(ws + 25 * MB);
    uint16_t* w2t = (uint16_t*)(ws + 26 * MB);
    float* csq = (float*)(ws + 28 * MB);
    float* cs1 = (float*)(ws + 28 * MB + 65536);
    uint16_t* out = (uint16_t*)d_out;
    constexpr float LN2 = 0.6931471805599453f;
    k_qkv1<<<256, 768, 0, stream>>>(x0, wqkv, nullptr, qkv, nullptr, csq, 1.0f, DM, 0, 0);
    k_attn<<<256, 768, 0, stream>>>(qkv, ob, wo, w1, w2, wot, w2t, 1, 0);
    k_oproj<<<256, 768, 0, stream>>>(ob, wot, x0, x1, st1, nullptr, 1.0f, LDA, DM, LDA);
    k_ffn1<<<224, 768, 0, stream>>>(x1, w1, b1, hb, st1, cs1, 1.0f, LDA, 0, 0);
    k_ffn2<<<256, 768, 0, stream>>>(hb, w2t, x1, x2, st2, nullptr, LN2, FF, LDA, LDA);
    k_qkv2<<<256, 768, 0, stream>>>(x2, wqkv, nullptr, qkv, st2, csq, 1.0f, LDA, 0, 0);
    k_attn<<<256, 768, 0, stream>>>(qkv, ob, wo, w1, w2, wot, w2t, 0, 0);
    k_oproj<<<256, 768, 0, stream>>>(ob, wot, x2, x1, st1, nullptr, 1.0f, LDA, LDA, LDA);
    k_ffn1<<<224, 768, 0, stream>>>(x1, w1, b1, hb, st1, cs1, 1.0f, LDA, 0, 0);
    k_ffn2<<<256, 768, 0, stream>>>(hb, w2t, x1, out, nullptr, nullptr, LN2, FF, LDA, DM);
}
```

```cpp
#include <hip/hip_runtime.h>
#include <hip/hip_bf16.h>
#include <stdint.h>

typedef __attribute__((ext_vector_type(8))) short bf16x8;
typedef __attribute__((ext_vector_type(4))) short s16x4;
typedef __attribute__((ext_vector_type(4))) float f32x4;
typedef __attribute__((ext_vector_type(2))) float f32x2;
typedef __attribute__((ext_vector_type(16))) float f32x16;
typedef __attribute__((ext_vector_type(4))) unsigned u32x4;
typedef __attribute__((ext_vector_type(2))) unsigned u32x2;
typedef __attribute__((ext_vector_type(2))) __bf16 bf16x2_t;
typedef __attribute__((address_space(3))) const char* lds_cptr;
typedef __attribute__((address_space(3))) char* lds_ptr;

#define DI __device__ __forceinline__
#define GAS __attribute__((address_space(1)))

constexpr int SEQ = 2048, DM = 512, NH = 32, FF = 1792;
constexpr int LDA = 576;

DI unsigned pack_bf16(float lo, float hi) {
    f32x2 v = {lo, hi};
    return __builtin_bit_cast(unsigned, __builtin_convertvector(v, bf16x2_t));
}
DI float bf_lo(unsigned u) { return __uint_as_float(u << 16); }
DI float bf_hi(unsigned u) { return __uint_as_float(u & 0xffff0000u); }
DI void glds16(unsigned voff, const void* sbase, unsigned lds_base) {
    asm volatile("s_mov_b32 m0, %2\n\ts_nop 0\n\tglobal_load_lds_dwordx4 %0, %1" ::"v"(voff), "s"(sbase), "s"(lds_base) : "memory");
}
DI void store16_wt(void* sbase, unsigned voff, u32x4 v) {
    asm volatile("global_store_dwordx4 %0, %1, %2 sc1\n\ts_nop 1" ::"v"(voff), "v"(v), "s"(sbase) : "memory");
}
DI u32x4 widen16(u32x2 a, u32x2 b) {
    auto x = __builtin_amdgcn_permlane16_swap(a[0], b[0], false, false);
    auto y = __builtin_amdgcn_permlane16_swap(a[1], b[1], false, false);
    return u32x4{x[0], y[0], x[1], y[1]};
}
DI float sum_rows(float v) {
    auto a = __builtin_amdgcn_permlane16_swap(__float_as_uint(v), __float_as_uint(v), false, false);
    v = __uint_as_float(a[0]) + __uint_as_float(a[1]);
    auto b = __builtin_amdgcn_permlane32_swap(__float_as_uint(v), __float_as_uint(v), false, false);
    return __uint_as_float(b[0]) + __uint_as_float(b[1]);
}
DI s16x4 lds_tr(lds_cptr p) {
    typedef short v4i16_t __attribute__((ext_vector_type(4)));
    return __builtin_bit_cast(s16x4, __builtin_amdgcn_ds_read_tr16_b64_v4i16((__attribute__((address_space(3))) v4i16_t*)p));
}
DI bf16x8 lds_b128(lds_cptr p) { return *(const __attribute__((address_space(3))) bf16x8*)p; }
template <int N> DI void wait_vm_bar() {
    __builtin_amdgcn_s_waitcnt(0xC07F);
    asm volatile("s_waitcnt vmcnt(%0) lgkmcnt(0)\n\ts_barrier" ::"i"(N) : "memory");
}

DI void bar_lds() {
    __builtin_amdgcn_s_waitcnt(0xC07F);
    asm volatile("s_waitcnt lgkmcnt(0)\n\ts_barrier" ::: "memory");
}

#define CODE_ADDR(sym) ({ unsigned long long a_;                                                                                  \
    asm volatile("s_getpc_b64 s[98:99]\n\ts_add_u32 s98, s98, " #sym "@rel32@lo+4\n\ts_addc_u32 s99, s99, " #sym "@rel32@hi+12\n\t" \
                 "s_mov_b64 %0, s[98:99]" : "=s"(a_) :: "scc", "s98", "s99"); (const char*)a_; })

#define MFMA16(a, b, c) __builtin_amdgcn_mfma_f32_16x16x32_bf16((a), (b), (c), 0, 0, 0)
#define MFMA32(a, b, c) __builtin_amdgcn_mfma_f32_32x32x16_bf16((a), (b), (c), 0, 0, 0)

enum { EPI_QKV = 0, EPI_FFN1 = 1, EPI_RES = 2 };
constexpr int LD_INIT = 2;

template <int PB> DI int wswz(int row) {
    if (PB == 64) return (row >> 3) & 1;
    if (PB == 128) return ((row >> 1) & 1) | (((row >> 3) & 1) << 1);
    return (row & 3) | (((row >> 3) & 1) << 2);
}

template <int EPI, int BM, int BN, int KT, int N, int WK, int NSLOT, int TMX  , bool SELF, bool CSIN>
DI void gemm_body(const uint16_t* X, const uint16_t* W, const uint16_t* aux  , uint16_t* out,
                  float* stats, float* csg, float oscale, int xp, int rp, int op  ,
                  const char* next_code, int next_kb) {
    asm volatile("" : "+s"(X), "+s"(W), "+s"(aux), "+s"(out), "+s"(stats), "+s"(csg), "+s"(oscale), "+s"(xp), "+s"(rp), "+s"(op));
    constexpr bool LN = (EPI != EPI_RES);
    constexpr int WP = LN ? N : BN;
    constexpr int TN = (WK == 1) ? BN / 32 : 2;
    constexpr int PB = (BN == 96) ? 64 : BN * 2;
    constexpr int SPR = PB / 32;
    constexpr int XB = BM * 128, WB = 64 * BN * 2;
    constexpr int SLOT = WK * (XB + WB);
    constexpr int NC = KT / (64 * WK);
    constexpr int PW = WK * WB / 4096;
    constexpr int NTN = N / BN, NTM = SEQ / BM;
    constexpr int RING = NSLOT * SLOT;
    constexpr int LDS_BYTES = RING + 3072;
    static_assert(WK * XB == 16384, "x pieces per slot must be 16");
    __shared__ __attribute__((aligned(16))) char lds[LDS_BYTES];

    const int tid = threadIdx.x, lane = tid & 63;
    const int wid = __builtin_amdgcn_readfirstlane(tid >> 6);
    int tm, tn;
    {
        const int x = blockIdx.x & 7, i = blockIdx.x >> 3;
        constexpr int XM = NTM / TMX;
        constexpr int XN = 8 / XM;
        constexpr int TNX = NTN / XN;
        tm = (x / XN) * TMX + (i % TMX);
        tn = (x % XN) * TNX + (i / TMX);
    }
    const int row0 = tm * BM, n0 = tn * BN;
    const unsigned lds0 = (unsigned)(uintptr_t)(lds_ptr)lds;

    if (__builtin_expect(wid < 4, 1)) {
        constexpr int PER = 4 + PW;
        const int l = wid;
        const int xr = 8 * l + (lane >> 3);
        constexpr bool XH = (EPI == EPI_RES && KT == 512);
        const int xc = (lane & 7) ^ ((xr >> 1) & 7);
        const unsigned xoff = XH ? (unsigned)((xc >> 1) * 65536 + xr * 32 + (xc & 1) * 16) : (unsigned)(xr * xp * 2 + (xc << 4));
        constexpr int RPP = 1024 / PB;
        const int wr = RPP * l + lane / (PB / 16), wc = lane % (PB / 16);
        const unsigned woff = (unsigned)(wr * WP * 2 + (((wc >> 1) ^ wswz<PB>(wr)) << 5) + ((wc & 1) << 4));
        constexpr int WSTEP = (BN == 96) ? 64 : 4 * RPP * WP * 2;
        const char* xg = (const char*)(X + (size_t)row0 * (XH ? 16 : xp));
        const char* wg = (const char*)(LN ? W + n0 : W + (size_t)tn * (KT * BN));
        const int xstep = XH ? 1024 : 64 * xp;
        constexpr int XK = XH ? 4 * 65536 : 128;
        auto issue = [&](int c) {
            const unsigned sb = lds0 + (c % NSLOT) * SLOT + l * 1024;
            const int kc = SELF ? ((c + tm) & (NC - 1)) : c;
            const char* xs = xg + (size_t)kc * (WK * XK);
            const char* ws = wg + (size_t)kc * (WK * 64 * WP * 2);
#pragma unroll
            for (int i = 0; i < 4; ++i)
                glds16(xoff, xs + ((WK == 1) ? i * xstep : (i & 1) * xstep + (i >> 1) * XK), sb + i * 4096);
#pragma unroll
            for (int i = 0; i < PW; ++i) glds16(woff, ws + i * WSTEP, sb + 16384 + i * 4096);
        };
#pragma unroll
        for (int c = 0; c < LD_INIT; ++c) issue(c);
#pragma unroll
        for (int b = -1; b < NC - 1; ++b) {
            constexpr auto nis = [](int bb) { int n = LD_INIT; for (int t = -1; t < bb; ++t) { int cap = t + NSLOT + 1; n = (n + 1 < cap) ? n + 1 : cap; if (n > NC) n = NC; } return n; };
            const int before = nis(b), after = nis(b + 1);
            const int rem = before - (b + 2);
            if (rem == 0) wait_vm_bar<0>(); else if (rem == 1) wait_vm_bar<PER>(); else if (rem == 2) wait_vm_bar<2 * PER>();
            else if (rem == 3) wait_vm_bar<3 * PER>(); else wait_vm_bar<4 * PER>();
#pragma unroll
            for (int c = 0; c < NC; ++c) if (c >= before && c < after) issue(c);
        }
        if (l == 0 && (int)(blockIdx.x >> 3) < next_kb)
            glds16(lane * 16, next_code + (blockIdx.x >> 3) * 1024, lds0 + RING + 2048);
        asm volatile("s_waitcnt vmcnt(0)" ::: "memory");
        return;
    }

    const int cw = wid - 4;
    const int r = lane & 15, g = lane >> 4;
    const int wk = (WK == 1) ? 0 : (cw >> 2);
    const int wm = (WK == 1) ? (cw >> 1) : ((cw >> 1) & 1);
    const int wn = cw & 1;
    const int mrot = LN ? wn : 0;
    const int jrot = LN ? (wm % TN) : 0;
    int jb[TN];
#pragma unroll
    for (int j = 0; j < TN; ++j) jb[j] = (j + jrot) % TN;
    const int mytok = row0 + wm * 32 + mrot * 16 + r;

    f32x4 pst;
    u32x2 auxv[TN];
    if (LN && !SELF) {
        pst = *(const GAS f32x4*)(stats + (size_t)mytok * 16 + g * 4);
    }
    f32x4 csv[TN];
    if (LN && CSIN) {
#pragma unroll
        for (int j = 0; j < TN; ++j) csv[j] = *(const GAS f32x4*)(csg + n0 + wn * (BN / 2) + jb[j] * 16 + g * 4);
    }
    if (EPI == EPI_FFN1) {
#pragma unroll
        for (int j = 0; j < TN; ++j) auxv[j] = *(const GAS u32x2*)(aux + n0 + wn * (BN / 2) + jb[j] * 16 + g * 4);
    }
    const int etok = row0 + cw * 8 + (lane >> 3);
    u32x4 resv;
    if (EPI == EPI_RES) resv = *(const GAS u32x4*)(aux + (size_t)etok * rp + n0 + (lane & 7) * 8);

    const int xrow = wm * 32 + r;
    const int xsw = (xrow >> 1) & 7;
    constexpr int NG = (NSLOT + 1) / 2;
    unsigned xa[NG][2][2], wa[NG][TN];
    {
        const int q = (lane >> 2) & 3, p = lane & 3;
        const int wrow = 8 * g + q;
#pragma unroll
        for (int gi = 0; gi < NG; ++gi) {
#pragma unroll
            for (int s = 0; s < 2; ++s)
#pragma unroll
                for (int m = 0; m < 2; ++m) {
                    xa[gi][s][m] = lds0 + gi * 2 * SLOT + wk * XB + (xrow + ((m ^ mrot) << 4)) * 128 + (((4 * s + g) ^ xsw) << 4);
                    asm volatile("" : "+v"(xa[gi][s][m]));
                }
#pragma unroll
            for (int j = 0; j < TN; ++j) {
                const int c16 = wn * TN + jb[j];
                wa[gi][j] = lds0 + gi * 2 * SLOT + WK * XB + wk * WB + (c16 / SPR) * (64 * PB) + wrow * PB +
                            (((c16 % SPR) ^ wswz<PB>(wrow)) << 5) + p * 8;
                asm volatile("" : "+v"(wa[gi][j]));
            }
        }
    }

    float* st = (float*)(lds + RING);
    float* cs = (float*)(lds + RING + 1024);
    auto put_stats = [&](float s1, float sq) {
        const float mu = s1 * (1.0f / KT);
        const float var = fmaxf(sq * (1.0f / KT) - mu * mu, 0.f);
        const float rs = __builtin_amdgcn_rsqf(var + 1e-5f);
        if (g == (r >> 2)) *(f32x2*)(st + (wm * 32 + mrot * 16 + r) * 2) = f32x2{mu * rs, rs};
    };

    f32x4 acc[2][TN];
#pragma unroll
    for (int m = 0; m < 2; ++m)
#pragma unroll
        for (int j = 0; j < TN; ++j) acc[m][j] = f32x4{0.f, 0.f, 0.f, 0.f};
    f32x4 xs1 = {0.f, 0.f, 0.f, 0.f}, xs2 = {0.f, 0.f, 0.f, 0.f}, wcs = {0.f, 0.f, 0.f, 0.f};
    const bf16x8 ones = {0x3f80, 0x3f80, 0x3f80, 0x3f80, 0x3f80, 0x3f80, 0x3f80, 0x3f80};

    bf16x8 xf[2][2][2], wf[2][2][TN];
    auto readf = [&](int c) {
        const int gi = (c % NSLOT) / 2, so = ((c % NSLOT) & 1) * SLOT, b = c & 1;
#pragma unroll
        for (int s = 0; s < 2; ++s) {
#pragma unroll
            for (int m = 0; m < 2; ++m) xf[b][s][m] = lds_b128((lds_cptr)(uintptr_t)xa[gi][s][m] + so);
#pragma unroll
            for (int j = 0; j < TN; ++j) {
                const s16x4 lo = lds_tr((lds_cptr)(uintptr_t)wa[gi][j] + so + (32 * s) * PB);
                const s16x4 hi = lds_tr((lds_cptr)(uintptr_t)wa[gi][j] + so + (32 * s + 4) * PB);
                wf[b][s][j] = __builtin_shufflevector(lo, hi, 0, 1, 2, 3, 4, 5, 6, 7);
            }
        }
    };
    auto fold_mfma = [&](int b) {
#pragma unroll
        for (int s = 0; s < 2; ++s) {
            if (LN && SELF) {
                xs1 = MFMA16(ones, xf[b][s][0], xs1);
                xs2 = MFMA16(xf[b][s][0], xf[b][s][0], xs2);
            }
            if (LN && !CSIN) wcs = MFMA16(wf[b][s][0], ones, wcs);
        }
    };
    auto main_mfma = [&](int b) {
#pragma unroll
        for (int s = 0; s < 2; ++s)
#pragma unroll
            for (int m = 0; m < 2; ++m)
#pragma unroll
                for (int j = 0; j < TN; ++j) acc[m][j] = MFMA16(wf[b][s][j], xf[b][s][m], acc[m][j]);
    };
    bar_lds();
    readf(0);
#pragma unroll
    for (int c = 0; c < NC - 1; ++c) {
        if (c == NC - 2) {
            main_mfma(c & 1);
            fold_mfma(c & 1);
            __builtin_amdgcn_sched_barrier(0);
            bar_lds();
            readf(c + 1);
            break;
        }
        bar_lds();
        __builtin_amdgcn_sched_barrier(0);
        readf(c + 1);
        main_mfma(c & 1);
        fold_mfma(c & 1);
        constexpr int NM = 2 * (2 * TN + ((LN && SELF) ? 2 : 0) + ((LN && !CSIN) ? 1 : 0)), NR = 2 * (2 + 2 * TN);
        constexpr int RPM = (WK == 1) ? 2 : 3;
#pragma unroll
        for (int i = 0; i < NM; ++i) {
            __builtin_amdgcn_sched_group_barrier(0x008, 1, 0);
            if (i * RPM < NR) __builtin_amdgcn_sched_group_barrier(0x100, RPM, 0);
        }
        __builtin_amdgcn_sched_barrier(0);
        if (LN && !SELF && c == NC - 3) {
            put_stats(sum_rows(pst[0] + pst[2]), sum_rows(pst[1] + pst[3]));
            __builtin_amdgcn_sched_barrier(0);
        }
    }
    fold_mfma((NC - 1) & 1);
    __builtin_amdgcn_sched_barrier(0);
    main_mfma((NC - 1) & 1);
    if (LN) {
        if (SELF) put_stats(xs1[0], (r & 2) ? ((r & 1) ? xs2[3] : xs2[2]) : ((r & 1) ? xs2[1] : xs2[0]));
        if (!CSIN && r == 0 && wm < TN) {
            *(f32x4*)(cs + wn * (BN / 2) + jrot * 16 + g * 4) = wcs;
            if (tm == 0) *(GAS f32x4*)(csg + n0 + wn * (BN / 2) + jrot * 16 + g * 4) = wcs;
        }
    }

    if (LN) {
        if (SELF || !CSIN) bar_lds();
        const unsigned stok = row0 + wm * 32 + (((g & 1) ^ mrot) << 4) + r;
        f32x2 ms[2];
#pragma unroll
        for (int m = 0; m < 2; ++m) ms[m] = *(const f32x2*)(st + (wm * 32 + ((m ^ mrot) << 4) + r) * 2);
#pragma unroll
        for (int j = 0; j < TN; ++j) {
            const int nl = wn * (BN / 2) + jb[j] * 16 + g * 4;
            const f32x4 c4 = CSIN ? csv[j] : *(const f32x4*)(cs + nl);
            if (EPI == EPI_QKV) {
                const int blk = (n0 + wn * (BN / 2)) / 16 + jb[j];
                const int sec = blk >> 5, head = blk & 31;
                const float sc = sec == 0 ? 0.25f * 1.4426950408889634f : 1.0f;
                char* base = (char*)out + (size_t)sec * (SEQ * DM * 2) + head * 65536;
                u32x2 o[2];
#pragma unroll
                for (int m = 0; m < 2; ++m) {
                    const float a = ms[m][1] * sc, bq = ms[m][0] * sc;
                    float v[4];
#pragma unroll
                    for (int e = 0; e < 4; ++e) v[e] = acc[m][j][e] * a - bq * c4[e];
                    o[m] = u32x2{pack_bf16(v[0], v[1]), pack_bf16(v[2], v[3])};
                }
                const unsigned off = sec < 2 ? (stok >> 5) * 1024 + (g >> 1) * 512 + (stok & 31) * 16 : stok * 32 + (g >> 1) * 16;
                store16_wt(base, off, widen16(o[0], o[1]));
            } else {
                constexpr float L2E = 1.4426950408889634f;
                const float bb[4] = {bf_lo(auxv[j][0]) * L2E, bf_hi(auxv[j][0]) * L2E, bf_lo(auxv[j][1]) * L2E, bf_hi(auxv[j][1]) * L2E};
                u32x2 o[2];
#pragma unroll
                for (int m = 0; m < 2; ++m) {
                    const float a = ms[m][1] * L2E, bq = ms[m][0] * L2E;
#pragma unroll
                    for (int e = 0; e < 4; e += 2) {
                        const f32x2 u = f32x2{acc[m][j][e], acc[m][j][e + 1]} * a + (f32x2{bb[e], bb[e + 1]} - f32x2{c4[e], c4[e + 1]} * bq);
                        const f32x2 ex = {__builtin_amdgcn_exp2f(u[0]), __builtin_amdgcn_exp2f(u[1])};
                        const f32x2 d = ex * (ex + 2.f) + 2.f;
                        const f32x2 rc = {__builtin_amdgcn_rcpf(d[0]), __builtin_amdgcn_rcpf(d[1])};
                        const f32x2 v = (u * rc) * -2.f + u;
                        o[m][e >> 1] = pack_bf16(v[0], v[1]);
                    }
                }
#pragma unroll
                for (int m = 0; m < 2; ++m)
                    *(u32x2*)(lds + cw * 4608 + ((((m ^ mrot) << 4) + r) * 144 + jb[j] * 32 + g * 8)) = o[m];
            }
        }
        if (EPI == EPI_FFN1) {
#pragma unroll
            for (int i = 0; i < 4; ++i) {
                const int row = i * 8 + (lane >> 3);
                const u32x4 v = *(const u32x4*)(lds + cw * 4608 + row * 144 + (lane & 7) * 16);
                store16_wt(out + n0 + wn * (BN / 2), (unsigned)((row0 + wm * 32 + row) * N + (lane & 7) * 8) * 2, v);
            }
        }
    } else {
        char* ex = lds + (((NC - 1) % NSLOT) == 3 ? 0 : 2 * SLOT);
#pragma unroll
        for (int m = 0; m < 2; ++m)
#pragma unroll
            for (int j = 0; j < 2; ++j)
                *(f32x4*)(ex + (wk * 64 + wm * 32 + m * 16 + r) * 272 + (wn * 32 + j * 16 + g * 4) * 4) = acc[m][j];
        bar_lds();
        const char* rd = ex + (cw * 8 + (lane >> 3)) * 272 + (lane & 7) * 32;
        const f32x4 a0 = *(const f32x4*)rd, a1 = *(const f32x4*)(rd + 16);
        const f32x4 b0 = *(const f32x4*)(rd + 64 * 272), b1 = *(const f32x4*)(rd + 64 * 272 + 16);
        u32x4 o;
        float s1 = 0.f, s2 = 0.f;
#pragma unroll
        for (int e = 0; e < 4; ++e) {
            const float lo = (e < 2 ? a0[2 * e] + b0[2 * e] : a1[2 * e - 4] + b1[2 * e - 4]);
            const float hi = (e < 2 ? a0[2 * e + 1] + b0[2 * e + 1] : a1[2 * e - 3] + b1[2 * e - 3]);
            o[e] = pack_bf16(fmaf(lo, oscale, bf_lo(resv[e])), fmaf(hi, oscale, bf_hi(resv[e])));
            const float q0 = bf_lo(o[e]), q1 = bf_hi(o[e]);
            s1 += q0 + q1;
            s2 = fmaf(q0, q0, fmaf(q1, q1, s2));
        }
        store16_wt(out + n0, (unsigned)(etok * op + (lane & 7) * 8) * 2, o);
        if (stats) {
#pragma unroll
            for (int d = 1; d < 8; d <<= 1) { s1 += __shfl_xor(s1, d); s2 += __shfl_xor(s2, d); }
            if ((lane & 7) == 0) *(GAS f32x2*)(stats + (size_t)etok * 16 + tn * 2) = f32x2{s1, s2};
        }
    }
}

constexpr int A_K = 0, A_V = 65536, A_ONE = 131072, A_Q = A_ONE + 8192, A_BYTES = A_Q + 8192;

constexpr unsigned PRIO_MASK = 0x8888u;
DI float max16(const f32x16& s) {
    float a = fmaxf(fmaxf(s[0], s[1]), s[2]), b = fmaxf(fmaxf(s[3], s[4]), s[5]);
    a = fmaxf(fmaxf(a, s[6]), s[7]); b = fmaxf(fmaxf(b, s[8]), s[9]);
    a = fmaxf(fmaxf(a, s[10]), s[11]); b = fmaxf(fmaxf(b, s[12]), s[13]);
    a = fmaxf(fmaxf(a, s[14]), s[15]);
    a = fmaxf(a, b);
    auto rr = __builtin_amdgcn_permlane32_swap(__float_as_uint(a), __float_as_uint(a), false, false);
    return fmaxf(__uint_as_float(rr[0]), __uint_as_float(rr[1]));
}

#define A_EXP(S, i) "v_exp_f32 v[" #S "+" #i "], v[" #S "+" #i "]\n\t"
#define A_CVT(P, S, i) "v_cvt_pk_bf16_f32 v[" #P "], v[" #S "+" #i "], v[" #S "+" #i "+1]\n\t"
#define A_TILE(SC, SN, U)                                                                              \
    "s_waitcnt lgkmcnt(4)\n\t"                                                                         \
    "v_mfma_f32_32x32x16_bf16 v[" #SN ":" #SN "+15], v[136:139], %[q], %[nm]\n\t"                      \
    "ds_read_b128 v[136:139], %[ka] offset:(" #U "+2)*1024\n\t"                                        \
    A_EXP(SC, 0) A_EXP(SC, 1) A_EXP(SC, 2) A_EXP(SC, 3) A_EXP(SC, 4) A_EXP(SC, 5) A_EXP(SC, 6) A_EXP(SC, 7) \
    A_CVT(128, SC, 0) A_CVT(129, SC, 2) A_CVT(130, SC, 4) A_CVT(131, SC, 6)                            \
    A_EXP(SC, 8) A_EXP(SC, 9)                                                                          \
    "s_waitcnt lgkmcnt(3)\n\t"                                                                         \
    "v_mfma_f32_32x32x16_bf16 %[o], v[140:143], v[128:131], %[o]\n\t"                                  \
    "ds_read_b64_tr_b16 v[140:141], %[va] offset:(" #U "+1)*1024\n\t"                                  \
    "ds_read_b64_tr_b16 v[142:143], %[va] offset:(" #U "+1)*1024+256\n\t"                              \
    A_EXP(SC, 10) A_EXP(SC, 11) A_EXP(SC, 12) A_EXP(SC, 13) A_EXP(SC, 14) A_EXP(SC, 15)                \
    A_CVT(132, SC, 8) A_CVT(133, SC, 10) A_CVT(134, SC, 12) A_CVT(135, SC, 14)                         \
    "s_waitcnt lgkmcnt(3)\n\t"                                                                         \
    "s_nop 0\n\t"                                                                                      \
    "v_mfma_f32_32x32x16_bf16 %[o], v[144:147], v[132:135], %[o]\n\t"                                  \
    "ds_read_b64_tr_b16 v[144:145], %[va] offset:(" #U "+1)*1024+512\n\t"                              \
    "ds_read_b64_tr_b16 v[146:147], %[va] offset:(" #U "+1)*1024+768\n\t"

DI void retile(const uint16_t* wo, const uint16_t* w2, uint16_t* wot, uint16_t* w2t, int gw, int lane) {
    const bool isw = gw < 512, third = gw < 256;
    const char* s0 = isw ? (const char*)wo + gw * 1024 : (const char*)w2 + (gw - 512) * 1024;
    char* d0 = isw ? (char*)wot + gw * 128 : (char*)w2t + (gw - 512) * 128;
    u32x4 v0 = *(const GAS u32x4*)(s0 + lane * 16);
    u32x4 v1 = *(const GAS u32x4*)((const char*)w2 + (gw + 512) * 1024 + lane * 16), v2;
    if (third) v2 = *(const GAS u32x4*)((const char*)w2 + (gw + 1536) * 1024 + lane * 16);
    const unsigned o2 = (unsigned)(lane >> 3) * (1792 * 128) + (lane & 7) * 16;
    store16_wt(d0, isw ? (unsigned)(lane >> 3) * (512 * 128) + (lane & 7) * 16 : o2, v0);
    store16_wt((char*)w2t + (gw + 512) * 128, o2, v1);
    if (third) store16_wt((char*)w2t + (gw + 1536) * 128, o2, v2);
}

extern "C" __global__ __launch_bounds__(768) void k_attn(const uint16_t* QKV, uint16_t* O, const uint16_t* wo, const uint16_t* w1,
                                                         const uint16_t* w2, uint16_t* wot, uint16_t* w2t, int first, int unused) {
    asm volatile("" : "+s"(QKV), "+s"(O));
    __shared__ __attribute__((aligned(16))) char lds[A_BYTES];
    const int tid = threadIdx.x, lane = tid & 63;
    const int wid = __builtin_amdgcn_readfirstlane(tid >> 6);
    const int x = blockIdx.x & 7, i = blockIdx.x >> 3;
    const int head = x * 4 + (i >> 3), qb = i & 7;
    const int r = lane & 31, h = lane >> 5;
    const unsigned lds0 = (unsigned)(uintptr_t)(lds_ptr)lds;
    const char* Qg = (const char*)QKV + head * 65536;
    const unsigned lo = lane * 16;
    if (__builtin_expect(wid >= 8, 0)) {
        const int l = wid - 8;
        const char* kv = Qg + (size_t)(1 + (l >> 1)) * (SEQ * DM * 2) + (l & 1) * 1024;
        const unsigned dst = lds0 + (l >> 1) * 65536 + (l & 1) * 1024;
        __builtin_amdgcn_s_setprio(3);
        asm volatile("s_barrier" ::: "memory");
#pragma unroll 2
        for (int p = 6; p < 32; ++p) glds16(lo, kv + p * 2048, dst + p * 2048);
        asm volatile("s_waitcnt vmcnt(0)\n\ts_barrier" ::: "memory");
        const int xw = i * 4 + l;
        const unsigned dummy = lds0 + A_Q + l * 1024;
        if (l == 0) {
            if (i < 3) glds16(lo, CODE_ADDR(k_oproj) + i * 1024, dummy);
            else if (i < 10) glds16(lo, CODE_ADDR(k_ffn1) + (i - 3) * 1024, dummy);
            else if (i < 17) glds16(lo, CODE_ADDR(k_ffn2) + (i - 10) * 1024, dummy);
            else if (i < 22) glds16(lo, CODE_ADDR(k_qkv2) + (i - 17) * 1024, dummy);
        }
        if (first) {
            retile(wo, w2, wot, w2t, x * 128 + xw, lane);
        } else {
            const char* a = (const char*)wot + (x & 1) * 262144 + xw * 2048;
            glds16(lo, a, dummy); glds16(lo, a + 1024, dummy);
            const char* c = (const char*)w2t + (x & 1) * 917504 + xw * 7168;
#pragma unroll
            for (int k = 0; k < 7; ++k) glds16(lo, c + k * 1024, dummy);
        }
        {
            const char* bw = (const char*)w1 + (x & 1) * 1792 + xw * (4 * 3584);
            const unsigned l56 = (lane < 56 ? lane : 55) * 16;
#pragma unroll
            for (int k = 0; k < 8; ++k) glds16(l56, bw + (k >> 1) * 3584 + (k & 1) * 896, dummy);
        }
        asm volatile("s_waitcnt vmcnt(0)" ::: "memory");
        return;
    }
    {
        const char* kv = Qg + (size_t)(1 + (wid & 1)) * (SEQ * DM * 2) + (wid >> 1) * 1024;
        const unsigned dst = lds0 + (wid & 1) * 65536 + (wid >> 1) * 1024;
        glds16(lo, Qg + (qb * 8 + wid) * 1024, lds0 + A_Q + wid * 1024);
#pragma unroll
        for (int p = 0; p < 3; ++p) glds16(lo, kv + p * 4096, dst + p * 4096);
    }
    *(u32x4*)(lds + A_ONE + tid * 16) = u32x4{(tid & 7) == 0 ? 0x00003f80u : 0u, 0u, 0u, 0u};

    const bool isv = (lane & 16) == 0;
    const unsigned ka0 = lds0 + A_K + h * 512 + r * 16;
    const unsigned va0 = isv ? lds0 + A_V + (4 * h + ((lane >> 2) & 3)) * 32 + (lane & 3) * 8
                             : lds0 + A_ONE + (1 - h) * 128 + ((lane & 3) ? 8 : 0);

    wait_vm_bar<0>();
    const bf16x8 qf = lds_b128((lds_cptr)lds + A_Q + wid * 1024 + h * 512 + r * 16);
    f32x16 negm;
    {
        f32x16 z = {};
        const f32x16 s0 = MFMA32(lds_b128((lds_cptr)(uintptr_t)ka0), qf, z);
        const float m0 = -max16(s0);
#pragma unroll
        for (int e = 0; e < 16; ++e) negm[e] = m0;
    }
    f32x16 o = {};
    {
        unsigned ka = ka0, va = va0;
        const unsigned vstep = isv ? 4096u : 0u;
        const unsigned pmask = wid < 4 ? PRIO_MASK : 0u;
        if (wid < 4) __builtin_amdgcn_s_setprio(2); else __builtin_amdgcn_s_setprio(1);
        int it;
        asm volatile(
            "ds_read_b128 v[136:139], %[ka]\n\t"
            "s_mov_b32 %[it], 0\n\t"
            "s_waitcnt lgkmcnt(0)\n\t"
            "v_mfma_f32_32x32x16_bf16 v[96:111], v[136:139], %[q], %[nm]\n\t"
            "ds_read_b128 v[136:139], %[ka] offset:1024\n\t"
            "ds_read_b64_tr_b16 v[140:141], %[va]\n\t"
            "ds_read_b64_tr_b16 v[142:143], %[va] offset:256\n\t"
            "ds_read_b64_tr_b16 v[144:145], %[va] offset:512\n\t"
            "ds_read_b64_tr_b16 v[146:147], %[va] offset:768\n\t"
            "s_nop 7\n\t"
            "1:\n\t"
            A_TILE(96, 112, 0) A_TILE(112, 96, 1) A_TILE(96, 112, 2) A_TILE(112, 96, 3)
            "v_add_u32 %[ka], 0x1000, %[ka]\n\t"
            "v_add_u32 %[va], %[vs], %[va]\n\t"
            "s_add_u32 %[it], %[it], 1\n\t"
            "s_cmp_lg_u32 %[it], 2\n\t"
            "s_cbranch_scc1 2f\n\t"
            "s_barrier\n\t"
            "2:\n\t"
            "s_cmp_eq_u32 %[pm], 0\n\t"
            "s_cbranch_scc1 3f\n\t"
            "s_bitcmp1_b32 %[pm], %[it]\n\t"
            "s_cbranch_scc1 4f\n\t"
            "s_setprio 2\n\t"
            "s_branch 3f\n\t"
            "4:\n\t"
            "s_setprio 0\n\t"
            "3:\n\t"
            "s_cmp_lg_u32 %[it], 16\n\t"
            "s_cbranch_scc1 1b\n\t"
            "s_waitcnt lgkmcnt(0)\n\t"
            "s_nop 15\n\t"
            : [o] "+v"(o), [ka] "+v"(ka), [va] "+v"(va), [it] "=&s"(it)
            : [q] "v"(qf), [nm] "v"(negm), [vs] "v"(vstep), [pm] "s"(pmask)
            : "memory", "scc", "v96", "v97", "v98", "v99", "v100", "v101", "v102", "v103", "v104", "v105", "v106", "v107",
              "v108", "v109", "v110", "v111", "v112", "v113", "v114", "v115", "v116", "v117", "v118", "v119", "v120", "v121",
              "v122", "v123", "v124", "v125", "v126", "v127", "v128", "v129", "v130", "v131", "v132", "v133", "v134", "v135",
              "v136", "v137", "v138", "v139", "v140", "v141", "v142", "v143", "v144", "v145", "v146", "v147");
    }

    bool bad = false;
#pragma unroll
    for (int e = 0; e < 9; ++e) bad |= !(__builtin_fabsf(o[e]) < __builtin_inff());
    if (__any(bad)) {
        lds_cptr k2 = (lds_cptr)(uintptr_t)ka0;
        lds_cptr v2 = (lds_cptr)(uintptr_t)va0;
        const int vs2 = isv ? 1024 : 0;
        float m = -__builtin_inff();
        o = f32x16{};
#pragma unroll 1
        for (int t = 0; t < 64; ++t) {
            f32x16 z = {};
            const f32x16 s = MFMA32(lds_b128(k2), qf, z);
            const float mn = fmaxf(m, max16(s));
            const float al = __builtin_amdgcn_exp2f(m - mn);
            m = mn;
#pragma unroll
            for (int e = 0; e < 16; ++e) o[e] *= al;
            u32x4 p0, p1;
#pragma unroll
            for (int e = 0; e < 4; ++e) {
                p0[e] = pack_bf16(__builtin_amdgcn_exp2f(s[2 * e] - mn), __builtin_amdgcn_exp2f(s[2 * e + 1] - mn));
                p1[e] = pack_bf16(__builtin_amdgcn_exp2f(s[8 + 2 * e] - mn), __builtin_amdgcn_exp2f(s[8 + 2 * e + 1] - mn));
            }
            const s16x4 v00 = lds_tr(v2), v01 = lds_tr(v2 + 256), v10 = lds_tr(v2 + 512), v11 = lds_tr(v2 + 768);
            o = MFMA32(__builtin_shufflevector(v00, v01, 0, 1, 2, 3, 4, 5, 6, 7), __builtin_bit_cast(bf16x8, p0), o);
            o = MFMA32(__builtin_shufflevector(v10, v11, 0, 1, 2, 3, 4, 5, 6, 7), __builtin_bit_cast(bf16x8, p1), o);
            k2 += 1024;
            v2 += vs2;
        }
    }
    auto lsw = __builtin_amdgcn_permlane32_swap(__float_as_uint(o[8]), __float_as_uint(o[8]), false, false);
    const float rl = 1.0f / __uint_as_float(lsw[0]);
    auto sx = __builtin_amdgcn_permlane32_swap(pack_bf16(o[0] * rl, o[1] * rl), pack_bf16(o[4] * rl, o[5] * rl), false, false);
    auto sy = __builtin_amdgcn_permlane32_swap(pack_bf16(o[2] * rl, o[3] * rl), pack_bf16(o[6] * rl, o[7] * rl), false, false);
    store16_wt((char*)O + head * 65536, (unsigned)((qb * 256 + wid * 32 + r) * 32 + h * 16), u32x4{sx[0], sy[0], sx[1], sy[1]});
}

#define GEMM_ARGS const uint16_t* X, const uint16_t* W, const uint16_t* aux, uint16_t* out, float* stats, float* csg, float oscale, int xp, int rp, int op
#define GEMM_PASS X, W, aux, out, stats, csg, oscale, xp, rp, op
extern "C" __global__ __launch_bounds__(768) void k_qkv1(GEMM_ARGS) {
    gemm_body<EPI_QKV, 128, 96, 512, 1536, 1, 5, 4, true, false>(GEMM_PASS, CODE_ADDR(k_attn), 4);
}
extern "C" __global__ __launch_bounds__(768) void k_qkv2(GEMM_ARGS) {
    gemm_body<EPI_QKV, 128, 96, 512, 1536, 1, 5, 4, false, true>(GEMM_PASS, CODE_ADDR(k_attn), 4);
}
extern "C" __global__ __launch_bounds__(768) void k_oproj(GEMM_ARGS) {
    gemm_body<EPI_RES, 64, 64, 512, 512, 2, 4, 8, false, false>(GEMM_PASS, CODE_ADDR(k_ffn1), 7);
}
extern "C" __global__ __launch_bounds__(768) void k_ffn1(GEMM_ARGS) {
    gemm_body<EPI_FFN1, 128, 128, 512, 1792, 1, 4, 4, false, false>(GEMM_PASS, CODE_ADDR(k_ffn2), 7);
}
extern "C" __global__ __launch_bounds__(768) void k_ffn2(GEMM_ARGS) {
    gemm_body<EPI_RES, 64, 64, 1792, 512, 2, 4, 8, false, false>(GEMM_PASS, CODE_ADDR(k_qkv2), 5);
}

extern "C" void kernel_launch(void* const* d_in, const int* in_sizes, int n_in, void* d_out, int out_size, void* d_ws,
                              size_t ws_size, hipStream_t stream) {
    const uint16_t* x0 = (const uint16_t*)d_in[0];
    const uint16_t* wqkv = (const uint16_t*)d_in[1];
    const uint16_t* wo = (const uint16_t*)d_in[2];
    const uint16_t* w1 = (const uint16_t*)d_in[4];
    const uint16_t* b1 = (const uint16_t*)d_in[5];
    const uint16_t* w2 = (const uint16_t*)d_in[6];
    char* ws = (char*)d_ws;
    constexpr size_t MB = 1u << 20;
    uint16_t* qkv = (uint16_t*)ws;
    uint16_t* ob = (uint16_t*)(ws + 6 * MB);
    uint16_t* x1 = (uint16_t*)(ws + 9 * MB);
    uint16_t* x2 = (uint16_t*)(ws + 12 * MB);
    uint16_t* hb = (uint16_t*)(ws + 15 * MB);
    float* st1 = (float*)(ws + 23 * MB);
    float* st2 = (float*)(ws + 24 * MB);
    uint16_t* wot = (uint16_t*)(ws + 25 * MB);
    uint16_t* w2t = (uint16_t*)(ws + 26 * MB);
    float* csq = (float*)(ws + 28 * MB);
    float* cs1 = (float*)(ws + 28 * MB + 65536);
    uint16_t* out = (uint16_t*)d_out;
    constexpr float LN2 = 0.6931471805599453f;
    k_qkv1<<<256, 768, 0, stream>>>(x0, wqkv, nullptr, qkv, nullptr, csq, 1.0f, DM, 0, 0);
    k_attn<<<256, 768, 0, stream>>>(qkv, ob, wo, w1, w2, wot, w2t, 1, 0);
    k_oproj<<<256, 768, 0, stream>>>(ob, wot, x0, x1, st1, nullptr, 1.0f, LDA, DM, LDA);
    k_ffn1<<<224, 768, 0, stream>>>(x1, w1, b1, hb, st1, cs1, 1.0f, LDA, 0, 0);
    k_ffn2<<<256, 768, 0, stream>>>(hb, w2t, x1, x2, st2, nullptr, LN2, FF, LDA, LDA);
    k_qkv2<<<256, 768, 0, stream>>>(x2, wqkv, nullptr, qkv, st2, csq, 1.0f, LDA, 0, 0);
    k_attn<<<256, 768, 0, stream>>>(qkv, ob, wo, w1, w2, wot, w2t, 0, 0);
    k_oproj<<<256, 768, 0, stream>>>(ob, wot, x2, x1, st1, nullptr, 1.0f, LDA, LDA, LDA);
    k_ffn1<<<224, 768, 0, stream>>>(x1, w1, b1, hb, st1, cs1, 1.0f, LDA, 0, 0);
    k_ffn2<<<256, 768, 0, stream>>>(hb, w2t, x1, out, nullptr, nullptr, LN2, FF, LDA, DM);
}
```

```cpp
#include <hip/hip_runtime.h>
#include <hip/hip_bf16.h>
#include <stdint.h>

typedef __attribute__((ext_vector_type(8))) short bf16x8;
typedef __attribute__((ext_vector_type(4))) short s16x4;
typedef __attribute__((ext_vector_type(4))) float f32x4;
typedef __attribute__((ext_vector_type(2))) float f32x2;
typedef __attribute__((ext_vector_type(16))) float f32x16;
typedef __attribute__((ext_vector_type(4))) unsigned u32x4;
typedef __attribute__((ext_vector_type(2))) unsigned u32x2;
typedef __attribute__((ext_vector_type(2))) __bf16 bf16x2_t;
typedef __attribute__((address_space(3))) const char* lds_cptr;
typedef __attribute__((address_space(3))) char* lds_ptr;

#define DI __device__ __forceinline__
#define GAS __attribute__((address_space(1)))

constexpr int SEQ = 2048, DM = 512, NH = 32, FF = 1792;
constexpr int LDA = 576;

DI unsigned pack_bf16(float lo, float hi) {
    f32x2 v = {lo, hi};
    return __builtin_bit_cast(unsigned, __builtin_convertvector(v, bf16x2_t));
}
DI float bf_lo(unsigned u) { return __uint_as_float(u << 16); }
DI float bf_hi(unsigned u) { return __uint_as_float(u & 0xffff0000u); }
DI void glds16(unsigned voff, const void* sbase, unsigned lds_base) {
    asm volatile("s_mov_b32 m0, %2\n\ts_nop 0\n\tglobal_load_lds_dwordx4 %0, %1" ::"v"(voff), "s"(sbase), "s"(lds_base) : "memory");
}
DI void store16_wt(void* sbase, unsigned voff, u32x4 v) {
    asm volatile("global_store_dwordx4 %0, %1, %2 sc1\n\ts_nop 1" ::"v"(voff), "v"(v), "s"(sbase) : "memory");
}
DI u32x4 widen16(u32x2 a, u32x2 b) {
    auto x = __builtin_amdgcn_permlane16_swap(a[0], b[0], false, false);
    auto y = __builtin_amdgcn_permlane16_swap(a[1], b[1], false, false);
    return u32x4{x[0], y[0], x[1], y[1]};
}
DI float sum_rows(float v) {
    auto a = __builtin_amdgcn_permlane16_swap(__float_as_uint(v), __float_as_uint(v), false, false);
    v = __uint_as_float(a[0]) + __uint_as_float(a[1]);
    auto b = __builtin_amdgcn_permlane32_swap(__float_as_uint(v), __float_as_uint(v), false, false);
    return __uint_as_float(b[0]) + __uint_as_float(b[1]);
}
template <int CTRL> DI float dpp_add(float v) {
    return v + __int_as_float(__builtin_amdgcn_update_dpp(0, __float_as_int(v), CTRL, 0xf, 0xf, true));
}
DI float sum8(float v) { return dpp_add<0x141>(dpp_add<0x4E>(dpp_add<0xB1>(v))); }
DI s16x4 lds_tr(lds_cptr p) {
    typedef short v4i16_t __attribute__((ext_vector_type(4)));
    return __builtin_bit_cast(s16x4, __builtin_amdgcn_ds_read_tr16_b64_v4i16((__attribute__((address_space(3))) v4i16_t*)p));
}
DI bf16x8 lds_b128(lds_cptr p) { return *(const __attribute__((address_space(3))) bf16x8*)p; }
template <int N> DI void wait_vm_bar() {
    __builtin_amdgcn_s_waitcnt(0xC07F);
    asm volatile("s_waitcnt vmcnt(%0) lgkmcnt(0)\n\ts_barrier" ::"i"(N) : "memory");
}

DI void bar_lds() {
    __builtin_amdgcn_s_waitcnt(0xC07F);
    asm volatile("s_waitcnt lgkmcnt(0)\n\ts_barrier" ::: "memory");
}

#define CODE_ADDR(sym) ({ unsigned long long a_;                                                                                  \
    asm volatile("s_getpc_b64 s[98:99]\n\ts_add_u32 s98, s98, " #sym "@rel32@lo+4\n\ts_addc_u32 s99, s99, " #sym "@rel32@hi+12\n\t" \
                 "s_mov_b64 %0, s[98:99]" : "=s"(a_) :: "scc", "s98", "s99"); (const char*)a_; })

#define MFMA16(a, b, c) __builtin_amdgcn_mfma_f32_16x16x32_bf16((a), (b), (c), 0, 0, 0)
#define MFMA32(a, b, c) __builtin_amdgcn_mfma_f32_32x32x16_bf16((a), (b), (c), 0, 0, 0)

enum { EPI_QKV = 0, EPI_FFN1 = 1, EPI_RES = 2 };
constexpr int LD_INIT = 2;

template <int PB> DI int wswz(int row) {
    if (PB == 64) return (row >> 3) & 1;
    if (PB == 128) return ((row >> 1) & 1) | (((row >> 3) & 1) << 1);
    return (row & 3) | (((row >> 3) & 1) << 2);
}

template <int EPI, int BM, int BN, int KT, int N, int WK, int NSLOT, int TMX  , bool SELF, bool CSIN>
DI void gemm_body(const uint16_t* X, const uint16_t* W, const uint16_t* aux  , uint16_t* out,
                  float* stats, float* csg, float oscale, int xp, int rp, int op  ,
                  const char* next_code, int next_kb) {
    asm volatile("" : "+s"(X), "+s"(W), "+s"(aux), "+s"(out), "+s"(stats), "+s"(csg), "+s"(oscale), "+s"(xp), "+s"(rp), "+s"(op));
    constexpr bool LN = (EPI != EPI_RES);
    constexpr int WP = LN ? N : BN;
    constexpr int TN = (WK == 1) ? BN / 32 : 2;
    constexpr int PB = (BN == 96) ? 64 : BN * 2;
    constexpr int SPR = PB / 32;
    constexpr int XB = BM * 128, WB = 64 * BN * 2;
    constexpr int SLOT = WK * (XB + WB);
    constexpr int NC = KT / (64 * WK);
    constexpr int PW = WK * WB / 4096;
    constexpr int NTN = N / BN, NTM = SEQ / BM;
    constexpr int RING = NSLOT * SLOT;
    constexpr int LDS_BYTES = RING + 3072;
    static_assert(WK * XB == 16384, "x pieces per slot must be 16");
    __shared__ __attribute__((aligned(16))) char lds[LDS_BYTES];

    const int tid = threadIdx.x, lane = tid & 63;
    const int wid = __builtin_amdgcn_readfirstlane(tid >> 6);
    int tm, tn;
    {
        const int x = blockIdx.x & 7, i = blockIdx.x >> 3;
        constexpr int XM = NTM / TMX;
        constexpr int XN = 8 / XM;
        constexpr int TNX = NTN / XN;
        tm = (x / XN) * TMX + (i % TMX);
        tn = (x % XN) * TNX + (i / TMX);
    }
    const int row0 = tm * BM, n0 = tn * BN;
    const unsigned lds0 = (unsigned)(uintptr_t)(lds_ptr)lds;

    if (__builtin_expect(wid < 4, 1)) {
        constexpr int PER = 4 + PW;
        const int l = wid;
        const int xr = 8 * l + (lane >> 3);
        constexpr bool XH = (EPI == EPI_RES && KT == 512);
        const int xc = (lane & 7) ^ ((xr >> 1) & 7);
        const unsigned xoff = XH ? (unsigned)((xc >> 1) * 65536 + xr * 32 + (xc & 1) * 16) : (unsigned)(xr * xp * 2 + (xc << 4));
        constexpr int RPP = 1024 / PB;
        const int wr = RPP * l + lane / (PB / 16), wc = lane % (PB / 16);
        const unsigned woff = (unsigned)(wr * WP * 2 + (((wc >> 1) ^ wswz<PB>(wr)) << 5) + ((wc & 1) << 4));
        constexpr int WSTEP = (BN == 96) ? 64 : 4 * RPP * WP * 2;
        const char* xg = (const char*)(X + (size_t)row0 * (XH ? 16 : xp));
        const char* wg = (const char*)(LN ? W + n0 : W + (size_t)tn * (KT * BN));
        const int xstep = XH ? 1024 : 64 * xp;
        constexpr int XK = XH ? 4 * 65536 : 128;
        auto issue = [&](int c) {
            const unsigned sb = lds0 + (c % NSLOT) * SLOT + l * 1024;
            const int kc = SELF ? ((c + tm) & (NC - 1)) : c;
            const char* xs = xg + (size_t)kc * (WK * XK);
            const char* ws = wg + (size_t)kc * (WK * 64 * WP * 2);
#pragma unroll
            for (int i = 0; i < 4; ++i)
                glds16(xoff, xs + ((WK == 1) ? i * xstep : (i & 1) * xstep + (i >> 1) * XK), sb + i * 4096);
#pragma unroll
            for (int i = 0; i < PW; ++i) glds16(woff, ws + i * WSTEP, sb + 16384 + i * 4096);
        };
#pragma unroll
        for (int c = 0; c < LD_INIT; ++c) issue(c);
#pragma unroll
        for (int b = -1; b < NC - 1; ++b) {
            constexpr auto nis = [](int bb) { int n = LD_INIT; for (int t = -1; t < bb; ++t) { int cap = t + NSLOT + 1; n = (n + 1 < cap) ? n + 1 : cap; if (n > NC) n = NC; } return n; };
            const int before = nis(b), after = nis(b + 1);
            const int rem = before - (b + 2);
            if (rem == 0) wait_vm_bar<0>(); else if (rem == 1) wait_vm_bar<PER>(); else if (rem == 2) wait_vm_bar<2 * PER>();
            else if (rem == 3) wait_vm_bar<3 * PER>(); else wait_vm_bar<4 * PER>();
#pragma unroll
            for (int c = 0; c < NC; ++c) if (c >= before && c < after) issue(c);
        }
        if (l == 0 && (int)(blockIdx.x >> 3) < next_kb)
            glds16(lane * 16, next_code + (blockIdx.x >> 3) * 1024, lds0 + RING + 2048);
        asm volatile("s_waitcnt vmcnt(0)" ::: "memory");
        return;
    }

    const int cw = wid - 4;
    const int r = lane & 15, g = lane >> 4;
    const int wk = (WK == 1) ? 0 : (cw >> 2);
    const int wm = (WK == 1) ? (cw >> 1) : ((cw >> 1) & 1);
    const int wn = cw & 1;
    const int mrot = LN ? wn : 0;
    const int jrot = LN ? (wm % TN) : 0;
    int jb[TN];
#pragma unroll
    for (int j = 0; j < TN; ++j) jb[j] = (j + jrot) % TN;
    const int mytok = row0 + wm * 32 + mrot * 16 + r;

    f32x4 pst;
    u32x2 auxv[TN];
    if (LN && !SELF) {
        pst = *(const GAS f32x4*)(stats + (size_t)mytok * 16 + g * 4);
    }
    f32x4 csv[TN];
    if (LN && CSIN) {
#pragma unroll
        for (int j = 0; j < TN; ++j) csv[j] = *(const GAS f32x4*)(csg + n0 + wn * (BN / 2) + jb[j] * 16 + g * 4);
    }
    if (EPI == EPI_FFN1) {
#pragma unroll
        for (int j = 0; j < TN; ++j) auxv[j] = *(const GAS u32x2*)(aux + n0 + wn * (BN / 2) + jb[j] * 16 + g * 4);
    }
    const int etok = row0 + cw * 8 + (lane >> 3);
    u32x4 resv;
    if (EPI == EPI_RES) resv = *(const GAS u32x4*)(aux + (size_t)etok * rp + n0 + (lane & 7) * 8);

    const int xrow = wm * 32 + r;
    const int xsw = (xrow >> 1) & 7;
    constexpr int NG = (NSLOT + 1) / 2;
    unsigned xa[NG][2][2], wa[NG][TN];
    {
        const int q = (lane >> 2) & 3, p = lane & 3;
        const int wrow = 8 * g + q;
#pragma unroll
        for (int gi = 0; gi < NG; ++gi) {
#pragma unroll
            for (int s = 0; s < 2; ++s)
#pragma unroll
                for (int m = 0; m < 2; ++m) {
                    xa[gi][s][m] = lds0 + gi * 2 * SLOT + wk * XB + (xrow + ((m ^ mrot) << 4)) * 128 + (((4 * s + g) ^ xsw) << 4);
                    asm volatile("" : "+v"(xa[gi][s][m]));
                }
#pragma unroll
            for (int j = 0; j < TN; ++j) {
                const int c16 = wn * TN + jb[j];
                wa[gi][j] = lds0 + gi * 2 * SLOT + WK * XB + wk * WB + (c16 / SPR) * (64 * PB) + wrow * PB +
                            (((c16 % SPR) ^ wswz<PB>(wrow)) << 5) + p * 8;
                asm volatile("" : "+v"(wa[gi][j]));
            }
        }
    }

    float* st = (float*)(lds + RING);
    float* cs = (float*)(lds + RING + 1024);
    auto put_stats = [&](float s1, float sq) {
        const float mu = s1 * (1.0f / KT);
        const float var = fmaxf(sq * (1.0f / KT) - mu * mu, 0.f);
        const float rs = __builtin_amdgcn_rsqf(var + 1e-5f);
        if (g == (r >> 2)) *(f32x2*)(st + (wm * 32 + mrot * 16 + r) * 2) = f32x2{mu * rs, rs};
    };

    f32x4 acc[2][TN];
#pragma unroll
    for (int m = 0; m < 2; ++m)
#pragma unroll
        for (int j = 0; j < TN; ++j) acc[m][j] = f32x4{0.f, 0.f, 0.f, 0.f};
    f32x4 xs1 = {0.f, 0.f, 0.f, 0.f}, xs2 = {0.f, 0.f, 0.f, 0.f}, wcs = {0.f, 0.f, 0.f, 0.f};
    const bf16x8 ones = {0x3f80, 0x3f80, 0x3f80, 0x3f80, 0x3f80, 0x3f80, 0x3f80, 0x3f80};

    bf16x8 xf[2][2][2], wf[2][2][TN];
    auto readf = [&](int c) {
        const int gi = (c % NSLOT) / 2, so = ((c % NSLOT) & 1) * SLOT, b = c & 1;
#pragma unroll
        for (int s = 0; s < 2; ++s) {
#pragma unroll
            for (int m = 0; m < 2; ++m) xf[b][s][m] = lds_b128((lds_cptr)(uintptr_t)xa[gi][s][m] + so);
#pragma unroll
            for (int j = 0; j < TN; ++j) {
                const s16x4 lo = lds_tr((lds_cptr)(uintptr_t)wa[gi][j] + so + (32 * s) * PB);
                const s16x4 hi = lds_tr((lds_cptr)(uintptr_t)wa[gi][j] + so + (32 * s + 4) * PB);
                wf[b][s][j] = __builtin_shufflevector(lo, hi, 0, 1, 2, 3, 4, 5, 6, 7);
            }
        }
    };
    auto fold_mfma = [&](int b) {
#pragma unroll
        for (int s = 0; s < 2; ++s) {
            if (LN && SELF) {
                xs1 = MFMA16(ones, xf[b][s][0], xs1);
                xs2 = MFMA16(xf[b][s][0], xf[b][s][0], xs2);
            }
            if (LN && !CSIN) wcs = MFMA16(wf[b][s][0], ones, wcs);
        }
    };
    auto main_mfma = [&](int b) {
#pragma unroll
        for (int s = 0; s < 2; ++s)
#pragma unroll
            for (int m = 0; m < 2; ++m)
#pragma unroll
                for (int j = 0; j < TN; ++j) acc[m][j] = MFMA16(wf[b][s][j], xf[b][s][m], acc[m][j]);
    };
    bar_lds();
    readf(0);
#pragma unroll
    for (int c = 0; c < NC - 1; ++c) {
        if (c == NC - 2) {
            main_mfma(c & 1);
            fold_mfma(c & 1);
            __builtin_amdgcn_sched_barrier(0);
            bar_lds();
            readf(c + 1);
            break;
        }
        bar_lds();
        __builtin_amdgcn_sched_barrier(0);
        readf(c + 1);
        main_mfma(c & 1);
        fold_mfma(c & 1);
        constexpr int NM = 2 * (2 * TN + ((LN && SELF) ? 2 : 0) + ((LN && !CSIN) ? 1 : 0)), NR = 2 * (2 + 2 * TN);
        constexpr int RPM = (WK == 1) ? 2 : 3;
#pragma unroll
        for (int i = 0; i < NM; ++i) {
            __builtin_amdgcn_sched_group_barrier(0x008, 1, 0);
            if (i * RPM < NR) __builtin_amdgcn_sched_group_barrier(0x100, RPM, 0);
        }
        __builtin_amdgcn_sched_barrier(0);
        if (LN && !SELF && c == NC - 3) {
            put_stats(sum_rows(pst[0] + pst[2]), sum_rows(pst[1] + pst[3]));
            __builtin_amdgcn_sched_barrier(0);
        }
    }
    fold_mfma((NC - 1) & 1);
    __builtin_amdgcn_sched_barrier(0);
    main_mfma((NC - 1) & 1);
    if (LN) {
        if (SELF) put_stats(xs1[0], (r & 2) ? ((r & 1) ? xs2[3] : xs2[2]) : ((r & 1) ? xs2[1] : xs2[0]));
        if (!CSIN && r == 0 && wm < TN) {
            *(f32x4*)(cs + wn * (BN / 2) + jrot * 16 + g * 4) = wcs;
            if (tm == 0) *(GAS f32x4*)(csg + n0 + wn * (BN / 2) + jrot * 16 + g * 4) = wcs;
        }
    }

    if (LN) {
        if (SELF || !CSIN) bar_lds();
        const unsigned stok = row0 + wm * 32 + (((g & 1) ^ mrot) << 4) + r;
        f32x2 ms[2];
#pragma unroll
        for (int m = 0; m < 2; ++m) ms[m] = *(const f32x2*)(st + (wm * 32 + ((m ^ mrot) << 4) + r) * 2);
#pragma unroll
        for (int j = 0; j < TN; ++j) {
            const int nl = wn * (BN / 2) + jb[j] * 16 + g * 4;
            const f32x4 c4 = CSIN ? csv[j] : *(const f32x4*)(cs + nl);
            if (EPI == EPI_QKV) {
                const int blk = (n0 + wn * (BN / 2)) / 16 + jb[j];
                const int sec = blk >> 5, head = blk & 31;
                const float sc = sec == 0 ? 0.25f * 1.4426950408889634f : 1.0f;
                char* base = (char*)out + (size_t)sec * (SEQ * DM * 2) + head * 65536;
                u32x2 o[2];
#pragma unroll
                for (int m = 0; m < 2; ++m) {
                    const float a = ms[m][1] * sc, bq = ms[m][0] * sc;
                    float v[4];
#pragma unroll
                    for (int e = 0; e < 4; ++e) v[e] = acc[m][j][e] * a - bq * c4[e];
                    o[m] = u32x2{pack_bf16(v[0], v[1]), pack_bf16(v[2], v[3])};
                }
                const unsigned off = sec < 2 ? (stok >> 5) * 1024 + (g >> 1) * 512 + (stok & 31) * 16 : stok * 32 + (g >> 1) * 16;
                store16_wt(base, off, widen16(o[0], o[1]));
            } else {
                constexpr float L2E = 1.4426950408889634f;
                const float bb[4] = {bf_lo(auxv[j][0]) * L2E, bf_hi(auxv[j][0]) * L2E, bf_lo(auxv[j][1]) * L2E, bf_hi(auxv[j][1]) * L2E};
                u32x2 o[2];
#pragma unroll
                for (int m = 0; m < 2; ++m) {
                    const float a = ms[m][1] * L2E, bq = ms[m][0] * L2E;
#pragma unroll
                    for (int e = 0; e < 4; e += 2) {
                        const f32x2 u = f32x2{acc[m][j][e], acc[m][j][e + 1]} * a + (f32x2{bb[e], bb[e + 1]} - f32x2{c4[e], c4[e + 1]} * bq);
                        const f32x2 ex = {__builtin_amdgcn_exp2f(u[0]), __builtin_amdgcn_exp2f(u[1])};
                        const f32x2 d = ex * (ex + 2.f) + 2.f;
                        const f32x2 rc = {__builtin_amdgcn_rcpf(d[0]), __builtin_amdgcn_rcpf(d[1])};
                        const f32x2 v = (u * rc) * -2.f + u;
                        o[m][e >> 1] = pack_bf16(v[0], v[1]);
                    }
                }
#pragma unroll
                for (int m = 0; m < 2; ++m)
                    *(u32x2*)(lds + cw * 4608 + ((((m ^ mrot) << 4) + r) * 144 + jb[j] * 32 + g * 8)) = o[m];
            }
        }
        if (EPI == EPI_FFN1) {
#pragma unroll
            for (int i = 0; i < 4; ++i) {
                const int row = i * 8 + (lane >> 3);
                const u32x4 v = *(const u32x4*)(lds + cw * 4608 + row * 144 + (lane & 7) * 16);
                store16_wt(out + n0 + wn * (BN / 2), (unsigned)((row0 + wm * 32 + row) * N + (lane & 7) * 8) * 2, v);
            }
        }
    } else {
        char* ex = lds + (((NC - 1) % NSLOT) == 3 ? 0 : 2 * SLOT);
#pragma unroll
        for (int m = 0; m < 2; ++m)
#pragma unroll
            for (int j = 0; j < 2; ++j)
                *(f32x4*)(ex + (wk * 64 + wm * 32 + m * 16 + r) * 272 + (wn * 32 + j * 16 + g * 4) * 4) = acc[m][j];
        bar_lds();
        const char* rd = ex + (cw * 8 + (lane >> 3)) * 272 + (lane & 7) * 32;
        const f32x4 a0 = *(const f32x4*)rd, a1 = *(const f32x4*)(rd + 16);
        const f32x4 b0 = *(const f32x4*)(rd + 64 * 272), b1 = *(const f32x4*)(rd + 64 * 272 + 16);
        u32x4 o;
        float s1 = 0.f, s2 = 0.f;
#pragma unroll
        for (int e = 0; e < 4; ++e) {
            const float lo = (e < 2 ? a0[2 * e] + b0[2 * e] : a1[2 * e - 4] + b1[2 * e - 4]);
            const float hi = (e < 2 ? a0[2 * e + 1] + b0[2 * e + 1] : a1[2 * e - 3] + b1[2 * e - 3]);
            o[e] = pack_bf16(fmaf(lo, oscale, bf_lo(resv[e])), fmaf(hi, oscale, bf_hi(resv[e])));
            const float q0 = bf_lo(o[e]), q1 = bf_hi(o[e]);
            s1 += q0 + q1;
            s2 = fmaf(q0, q0, fmaf(q1, q1, s2));
        }
        store16_wt(out + n0, (unsigned)(etok * op + (lane & 7) * 8) * 2, o);
        if (stats) {
            s1 = sum8(s1); s2 = sum8(s2);
            if ((lane & 7) == 0) *(GAS f32x2*)(stats + (size_t)etok * 16 + tn * 2) = f32x2{s1, s2};
        }
    }
}

constexpr int A_K = 0, A_V = 65536, A_ONE = 131072, A_Q = A_ONE + 8192, A_BYTES = A_Q + 8192;

constexpr unsigned PRIO_MASK = 0x8888u;
DI float max16(const f32x16& s) {
    float a = fmaxf(fmaxf(s[0], s[1]), s[2]), b = fmaxf(fmaxf(s[3], s[4]), s[5]);
    a = fmaxf(fmaxf(a, s[6]), s[7]); b = fmaxf(fmaxf(b, s[8]), s[9]);
    a = fmaxf(fmaxf(a, s[10]), s[11]); b = fmaxf(fmaxf(b, s[12]), s[13]);
    a = fmaxf(fmaxf(a, s[14]), s[15]);
    a = fmaxf(a, b);
    auto rr = __builtin_amdgcn_permlane32_swap(__float_as_uint(a), __float_as_uint(a), false, false);
    return fmaxf(__uint_as_float(rr[0]), __uint_as_float(rr[1]));
}

#define A_EXP(S, i) "v_exp_f32 v[" #S "+" #i "], v[" #S "+" #i "]\n\t"
#define A_CVT(P, S, i) "v_cvt_pk_bf16_f32 v[" #P "], v[" #S "+" #i "], v[" #S "+" #i "+1]\n\t"
#define A_TILE(SC, SN, U)                                                                              \
    "s_waitcnt lgkmcnt(4)\n\t"                                                                         \
    "v_mfma_f32_32x32x16_bf16 v[" #SN ":" #SN "+15], v[136:139], %[q], %[nm]\n\t"                      \
    "ds_read_b128 v[136:139], %[ka] offset:(" #U "+2)*1024\n\t"                                        \
    A_EXP(SC, 0) A_EXP(SC, 1) A_EXP(SC, 2) A_EXP(SC, 3) A_EXP(SC, 4) A_EXP(SC, 5) A_EXP(SC, 6) A_EXP(SC, 7) \
    A_CVT(128, SC, 0) A_CVT(129, SC, 2) A_CVT(130, SC, 4) A_CVT(131, SC, 6)                            \
    A_EXP(SC, 8) A_EXP(SC, 9)                                                                          \
    "s_waitcnt lgkmcnt(3)\n\t"                                                                         \
    "v_mfma_f32_32x32x16_bf16 %[o], v[140:143], v[128:131], %[o]\n\t"                                  \
    "ds_read_b64_tr_b16 v[140:141], %[va] offset:(" #U "+1)*1024\n\t"                                  \
    "ds_read_b64_tr_b16 v[142:143], %[va] offset:(" #U "+1)*1024+256\n\t"                              \
    A_EXP(SC, 10) A_EXP(SC, 11) A_EXP(SC, 12) A_EXP(SC, 13) A_EXP(SC, 14) A_EXP(SC, 15)                \
    A_CVT(132, SC, 8) A_CVT(133, SC, 10) A_CVT(134, SC, 12) A_CVT(135, SC, 14)                         \
    "s_waitcnt lgkmcnt(3)\n\t"                                                                         \
    "s_nop 0\n\t"                                                                                      \
    "v_mfma_f32_32x32x16_bf16 %[o], v[144:147], v[132:135], %[o]\n\t"                                  \
    "ds_read_b64_tr_b16 v[144:145], %[va] offset:(" #U "+1)*1024+512\n\t"                              \
    "ds_read_b64_tr_b16 v[146:147], %[va] offset:(" #U "+1)*1024+768\n\t"

DI void retile(const uint16_t* wo, const uint16_t* w2, uint16_t* wot, uint16_t* w2t, int gw, int lane) {
    const bool isw = gw < 512, third = gw < 256;
    const char* s0 = isw ? (const char*)wo + gw * 1024 : (const char*)w2 + (gw - 512) * 1024;
    char* d0 = isw ? (char*)wot + gw * 128 : (char*)w2t + (gw - 512) * 128;
    u32x4 v0 = *(const GAS u32x4*)(s0 + lane * 16);
    u32x4 v1 = *(const GAS u32x4*)((const char*)w2 + (gw + 512) * 1024 + lane * 16), v2;
    if (third) v2 = *(const GAS u32x4*)((const char*)w2 + (gw + 1536) * 1024 + lane * 16);
    const unsigned o2 = (unsigned)(lane >> 3) * (1792 * 128) + (lane & 7) * 16;
    store16_wt(d0, isw ? (unsigned)(lane >> 3) * (512 * 128) + (lane & 7) * 16 : o2, v0);
    store16_wt((char*)w2t + (gw + 512) * 128, o2, v1);
    if (third) store16_wt((char*)w2t + (gw + 1536) * 128, o2, v2);
}

extern "C" __global__ __launch_bounds__(768) void k_attn(const uint16_t* QKV, uint16_t* O, const uint16_t* wo, const uint16_t* w1,
                                                         const uint16_t* w2, uint16_t* wot, uint16_t* w2t, int first, int unused) {
    asm volatile("" : "+s"(QKV), "+s"(O));
    __shared__ __attribute__((aligned(16))) char lds[A_BYTES];
    const int tid = threadIdx.x, lane = tid & 63;
    const int wid = __builtin_amdgcn_readfirstlane(tid >> 6);
    const int x = blockIdx.x & 7, i = blockIdx.x >> 3;
    const int head = x * 4 + (i >> 3), qb = i & 7;
    const int r = lane & 31, h = lane >> 5;
    const unsigned lds0 = (unsigned)(uintptr_t)(lds_ptr)lds;
    const char* Qg = (const char*)QKV + head * 65536;
    const unsigned lo = lane * 16;
    if (__builtin_expect(wid >= 8, 0)) {
        const int l = wid - 8;
        const char* kv = Qg + (size_t)(1 + (l >> 1)) * (SEQ * DM * 2) + (l & 1) * 1024;
        const unsigned dst = lds0 + (l >> 1) * 65536 + (l & 1) * 1024;
        __builtin_amdgcn_s_setprio(3);
        asm volatile("s_barrier" ::: "memory");
#pragma unroll 2
        for (int p = 6; p < 32; ++p) glds16(lo, kv + p * 2048, dst + p * 2048);
        asm volatile("s_waitcnt vmcnt(0)\n\ts_barrier" ::: "memory");
        const int xw = i * 4 + l;
        const unsigned dummy = lds0 + A_Q + l * 1024;
        if (l == 0) {
            if (i < 3) glds16(lo, CODE_ADDR(k_oproj) + i * 1024, dummy);
            else if (i < 10) glds16(lo, CODE_ADDR(k_ffn1) + (i - 3) * 1024, dummy);
            else if (i < 17) glds16(lo, CODE_ADDR(k_ffn2) + (i - 10) * 1024, dummy);
            else if (i < 22) glds16(lo, CODE_ADDR(k_qkv2) + (i - 17) * 1024, dummy);
        }
        if (first) {
            retile(wo, w2, wot, w2t, x * 128 + xw, lane);
        } else {
            const char* a = (const char*)wot + (x & 1) * 262144 + xw * 2048;
            glds16(lo, a, dummy); glds16(lo, a + 1024, dummy);
            const char* c = (const char*)w2t + (x & 1) * 917504 + xw * 7168;
#pragma unroll
            for (int k = 0; k < 7; ++k) glds16(lo, c + k * 1024, dummy);
        }
        {
            const char* bw = (const char*)w1 + (x & 1) * 1792 + xw * (4 * 3584);
            const unsigned l56 = (lane < 56 ? lane : 55) * 16;
#pragma unroll
            for (int k = 0; k < 8; ++k) glds16(l56, bw + (k >> 1) * 3584 + (k & 1) * 896, dummy);
        }
        asm volatile("s_waitcnt vmcnt(0)" ::: "memory");
        return;
    }
    {
        const char* kv = Qg + (size_t)(1 + (wid & 1)) * (SEQ * DM * 2) + (wid >> 1) * 1024;
        const unsigned dst = lds0 + (wid & 1) * 65536 + (wid >> 1) * 1024;
        glds16(lo, Qg + (qb * 8 + wid) * 1024, lds0 + A_Q + wid * 1024);
#pragma unroll
        for (int p = 0; p < 3; ++p) glds16(lo, kv + p * 4096, dst + p * 4096);
    }
    *(u32x4*)(lds + A_ONE + tid * 16) = u32x4{(tid & 7) == 0 ? 0x00003f80u : 0u, 0u, 0u, 0u};

    const bool isv = (lane & 16) == 0;
    const unsigned ka0 = lds0 + A_K + h * 512 + r * 16;
    const unsigned va0 = isv ? lds0 + A_V + (4 * h + ((lane >> 2) & 3)) * 32 + (lane & 3) * 8
                             : lds0 + A_ONE + (1 - h) * 128 + ((lane & 3) ? 8 : 0);

    wait_vm_bar<0>();
    const bf16x8 qf = lds_b128((lds_cptr)lds + A_Q + wid * 1024 + h * 512 + r * 16);
    f32x16 negm;
    {
        f32x16 z = {};
        const f32x16 s0 = MFMA32(lds_b128((lds_cptr)(uintptr_t)ka0), qf, z);
        const float m0 = -max16(s0);
#pragma unroll
        for (int e = 0; e < 16; ++e) negm[e] = m0;
    }
    f32x16 o = {};
    {
        unsigned ka = ka0, va = va0;
        const unsigned vstep = isv ? 4096u : 0u;
        const unsigned pmask = wid < 4 ? PRIO_MASK : 0u;
        if (wid < 4) __builtin_amdgcn_s_setprio(2); else __builtin_amdgcn_s_setprio(1);
        int it;
        asm volatile(
            "ds_read_b128 v[136:139], %[ka]\n\t"
            "s_mov_b32 %[it], 0\n\t"
            "s_waitcnt lgkmcnt(0)\n\t"
            "v_mfma_f32_32x32x16_bf16 v[96:111], v[136:139], %[q], %[nm]\n\t"
            "ds_read_b128 v[136:139], %[ka] offset:1024\n\t"
            "ds_read_b64_tr_b16 v[140:141], %[va]\n\t"
            "ds_read_b64_tr_b16 v[142:143], %[va] offset:256\n\t"
            "ds_read_b64_tr_b16 v[144:145], %[va] offset:512\n\t"
            "ds_read_b64_tr_b16 v[146:147], %[va] offset:768\n\t"
            "s_nop 7\n\t"
            "1:\n\t"
            A_TILE(96, 112, 0) A_TILE(112, 96, 1) A_TILE(96, 112, 2) A_TILE(112, 96, 3)
            "v_add_u32 %[ka], 0x1000, %[ka]\n\t"
            "v_add_u32 %[va], %[vs], %[va]\n\t"
            "s_add_u32 %[it], %[it], 1\n\t"
            "s_cmp_lg_u32 %[it], 2\n\t"
            "s_cbranch_scc1 2f\n\t"
            "s_barrier\n\t"
            "2:\n\t"
            "s_cmp_eq_u32 %[pm], 0\n\t"
            "s_cbranch_scc1 3f\n\t"
            "s_bitcmp1_b32 %[pm], %[it]\n\t"
            "s_cbranch_scc1 4f\n\t"
            "s_setprio 2\n\t"
            "s_branch 3f\n\t"
            "4:\n\t"
            "s_setprio 0\n\t"
            "3:\n\t"
            "s_cmp_lg_u32 %[it], 16\n\t"
            "s_cbranch_scc1 1b\n\t"
            "s_waitcnt lgkmcnt(0)\n\t"
            "s_nop 15\n\t"
            : [o] "+v"(o), [ka] "+v"(ka), [va] "+v"(va), [it] "=&s"(it)
            : [q] "v"(qf), [nm] "v"(negm), [vs] "v"(vstep), [pm] "s"(pmask)
            : "memory", "scc", "v96", "v97", "v98", "v99", "v100", "v101", "v102", "v103", "v104", "v105", "v106", "v107",
              "v108", "v109", "v110", "v111", "v112", "v113", "v114", "v115", "v116", "v117", "v118", "v119", "v120", "v121",
              "v122", "v123", "v124", "v125", "v126", "v127", "v128", "v129", "v130", "v131", "v132", "v133", "v134", "v135",
              "v136", "v137", "v138", "v139", "v140", "v141", "v142", "v143", "v144", "v145", "v146", "v147");
    }

    bool bad = false;
#pragma unroll
    for (int e = 0; e < 9; ++e) bad |= !(__builtin_fabsf(o[e]) < __builtin_inff());
    if (__any(bad)) {
        lds_cptr k2 = (lds_cptr)(uintptr_t)ka0;
        lds_cptr v2 = (lds_cptr)(uintptr_t)va0;
        const int vs2 = isv ? 1024 : 0;
        float m = -__builtin_inff();
        o = f32x16{};
#pragma unroll 1
        for (int t = 0; t < 64; ++t) {
            f32x16 z = {};
            const f32x16 s = MFMA32(lds_b128(k2), qf, z);
            const float mn = fmaxf(m, max16(s));
            const float al = __builtin_amdgcn_exp2f(m - mn);
            m = mn;
#pragma unroll
            for (int e = 0; e < 16; ++e) o[e] *= al;
            u32x4 p0, p1;
#pragma unroll
            for (int e = 0; e < 4; ++e) {
                p0[e] = pack_bf16(__builtin_amdgcn_exp2f(s[2 * e] - mn), __builtin_amdgcn_exp2f(s[2 * e + 1] - mn));
                p1[e] = pack_bf16(__builtin_amdgcn_exp2f(s[8 + 2 * e] - mn), __builtin_amdgcn_exp2f(s[8 + 2 * e + 1] - mn));
            }
            const s16x4 v00 = lds_tr(v2), v01 = lds_tr(v2 + 256), v10 = lds_tr(v2 + 512), v11 = lds_tr(v2 + 768);
            o = MFMA32(__builtin_shufflevector(v00, v01, 0, 1, 2, 3, 4, 5, 6, 7), __builtin_bit_cast(bf16x8, p0), o);
            o = MFMA32(__builtin_shufflevector(v10, v11, 0, 1, 2, 3, 4, 5, 6, 7), __builtin_bit_cast(bf16x8, p1), o);
            k2 += 1024;
            v2 += vs2;
        }
    }
    auto lsw = __builtin_amdgcn_permlane32_swap(__float_as_uint(o[8]), __float_as_uint(o[8]), false, false);
    const float rl = 1.0f / __uint_as_float(lsw[0]);
    auto sx = __builtin_amdgcn_permlane32_swap(pack_bf16(o[0] * rl, o[1] * rl), pack_bf16(o[4] * rl, o[5] * rl), false, false);
    auto sy = __builtin_amdgcn_permlane32_swap(pack_bf16(o[2] * rl, o[3] * rl), pack_bf16(o[6] * rl, o[7] * rl), false, false);
    store16_wt((char*)O + head * 65536, (unsigned)((qb * 256 + wid * 32 + r) * 32 + h * 16), u32x4{sx[0], sy[0], sx[1], sy[1]});
}

#define GEMM_ARGS const uint16_t* X, const uint16_t* W, const uint16_t* aux, uint16_t* out, float* stats, float* csg, float oscale, int xp, int rp, int op
#define GEMM_PASS X, W, aux, out, stats, csg, oscale, xp, rp, op
extern "C" __global__ __launch_bounds__(768) void k_qkv1(GEMM_ARGS) {
    gemm_body<EPI_QKV, 128, 96, 512, 1536, 1, 5, 4, true, false>(GEMM_PASS, CODE_ADDR(k_attn), 4);
}
extern "C" __global__ __launch_bounds__(768) void k_qkv2(GEMM_ARGS) {
    gemm_body<EPI_QKV, 128, 96, 512, 1536, 1, 5, 4, false, true>(GEMM_PASS, CODE_ADDR(k_attn), 4);
}
extern "C" __global__ __launch_bounds__(768) void k_oproj(GEMM_ARGS) {
    gemm_body<EPI_RES, 64, 64, 512, 512, 2, 4, 8, false, false>(GEMM_PASS, CODE_ADDR(k_ffn1), 7);
}
extern "C" __global__ __launch_bounds__(768) void k_ffn1(GEMM_ARGS) {
    gemm_body<EPI_FFN1, 128, 128, 512, 1792, 1, 4, 4, false, false>(GEMM_PASS, CODE_ADDR(k_ffn2), 7);
}
extern "C" __global__ __launch_bounds__(768) void k_ffn2(GEMM_ARGS) {
    gemm_body<EPI_RES, 64, 64, 1792, 512, 2, 4, 8, false, false>(GEMM_PASS, CODE_ADDR(k_qkv2), 5);
}

extern "C" void kernel_launch(void* const* d_in, const int* in_sizes, int n_in, void* d_out, int out_size, void* d_ws,
                              size_t ws_size, hipStream_t stream) {
    const uint16_t* x0 = (const uint16_t*)d_in[0];
    const uint16_t* wqkv = (const uint16_t*)d_in[1];
    const uint16_t* wo = (const uint16_t*)d_in[2];
    const uint16_t* w1 = (const uint16_t*)d_in[4];
    const uint16_t* b1 = (const uint16_t*)d_in[5];
    const uint16_t* w2 = (const uint16_t*)d_in[6];
    char* ws = (char*)d_ws;
    constexpr size_t MB = 1u << 20;
    uint16_t* qkv = (uint16_t*)ws;
    uint16_t* ob = (uint16_t*)(ws + 6 * MB);
    uint16_t* x1 = (uint16_t*)(ws + 9 * MB);
    uint16_t* x2 = (uint16_t*)(ws + 12 * MB);
    uint16_t* hb = (uint16_t*)(ws + 15 * MB);
    float* st1 = (float*)(ws + 23 * MB);
    float* st2 = (float*)(ws + 24 * MB);
    uint16_t* wot = (uint16_t*)(ws + 25 * MB);
    uint16_t* w2t = (uint16_t*)(ws + 26 * MB);
    float* csq = (float*)(ws + 28 * MB);
    float* cs1 = (float*)(ws + 28 * MB + 65536);
    uint16_t* out = (uint16_t*)d_out;
    constexpr float LN2 = 0.6931471805599453f;
    k_qkv1<<<256, 768, 0, stream>>>(x0, wqkv, nullptr, qkv, nullptr, csq, 1.0f, DM, 0, 0);
    k_attn<<<256, 768, 0, stream>>>(qkv, ob, wo, w1, w2, wot, w2t, 1, 0);
    k_oproj<<<256, 768, 0, stream>>>(ob, wot, x0, x1, st1, nullptr, 1.0f, LDA, DM, LDA);
    k_ffn1<<<224, 768, 0, stream>>>(x1, w1, b1, hb, st1, cs1, 1.0f, LDA, 0, 0);
    k_ffn2<<<256, 768, 0, stream>>>(hb, w2t, x1, x2, st2, nullptr, LN2, FF, LDA, LDA);
    k_qkv2<<<256, 768, 0, stream>>>(x2, wqkv, nullptr, qkv, st2, csq, 1.0f, LDA, 0, 0);
    k_attn<<<256, 768, 0, stream>>>(qkv, ob, wo, w1, w2, wot, w2t, 0, 0);
    k_oproj<<<256, 768, 0, stream>>>(ob, wot, x2, x1, st1, nullptr, 1.0f, LDA, LDA, LDA);
    k_ffn1<<<224, 768, 0, stream>>>(x1, w1, b1, hb, st1, cs1, 1.0f, LDA, 0, 0);
    k_ffn2<<<256, 768, 0, stream>>>(hb, w2t, x1, out, nullptr, nullptr, LN2, FF, LDA, DM);
}
```
